```python
import torch

def _softmax(x, axis=-1):
    return torch.softmax(x, dim=axis)

def _gelu(x):
    return torch.nn.functional.gelu(x, approximate='tanh')

def _silu(x):
    return torch.nn.functional.silu(x)

def _sigmoid(x):
    return torch.sigmoid(x)

def _layernorm(x, axis=-1, eps=1e-05):
    m = x.mean(dim=axis, keepdim=True)
    v = x.var(dim=axis, keepdim=True, unbiased=False)
    return (x - m) / torch.sqrt(v + eps)

def _scatter_add(tgt, idx, src, axis=0):
    return tgt.clone().index_add_(axis, idx, src)

def _segment_sum(src, seg, n):
    out = torch.zeros((n,) + src.shape[1:], dtype=src.dtype)
    return out.index_add_(0, seg, src)

def _pad(x, pw):
    flat = tuple((v for pair in reversed(pw) for v in pair))
    return torch.nn.functional.pad(x, flat)
kernel_name = 'adaLN_cond__gqa_block_cached__glu_gated_geglu__prompt'

def setup_inputs():

    _w = lambda a: (lambda t: t.to(torch.bfloat16) if t.is_floating_point() else t)(torch.as_tensor(a))

    def _A_setup():
        B, Sq, D = (1, 2048, 512)
        Hq, Hkv, Sk, Dh = (32, 32, 2048, 128)
        assert Hq % Hkv == 0, (Hq, Hkv)
        return {'x': _w((lambda *s: torch.randn(*s, dtype=torch.bfloat16) * 0.05)(B, Sq, D)), 'k_cache': _w((lambda *s: torch.randn(*s, dtype=torch.bfloat16) * 0.8)(B, Hkv, Sk, Dh)), 'v_cache': _w((lambda *s: torch.randn(*s, dtype=torch.bfloat16) * 3.2)(B, Hkv, Sk, Dh)), 'wq': _w((lambda *s: torch.randn(*s, dtype=torch.bfloat16) * 0.8)(D, Hq * Dh) * D ** (-0.5)), 'wo': _w((lambda *s: torch.randn(*s, dtype=torch.bfloat16) * 3.2)(Hq * Dh, D) * (Hq * Dh) ** (-0.5))}
    _A_in = _A_setup()

    def _B_setup():
        B, S, Dm, Df = (1, 2048, 512, 1376)
        return {'x': _w((lambda *s: torch.randn(*s, dtype=torch.bfloat16) * 0.05)(B, S, Dm)), 'w_gate': _w((lambda *s: torch.randn(*s, dtype=torch.bfloat16) * 3.2)(Dm, Df) * Dm ** (-0.5)), 'w_up': _w((lambda *s: torch.randn(*s, dtype=torch.bfloat16) * 3.2)(Dm, Df) * Dm ** (-0.5)), 'w_down': _w((lambda *s: torch.randn(*s, dtype=torch.bfloat16) * 3.2)(Df, Dm) * Df ** (-0.5))}
    _B_in = _B_setup()
    return {'A_x': _A_in['x'], 'A_k_cache': _A_in['k_cache'], 'A_v_cache': _A_in['v_cache'], 'A_wq': _A_in['wq'], 'A_wo': _A_in['wo'], 'B_x': _B_in['x'], 'B_w_gate': _B_in['w_gate'], 'B_w_up': _B_in['w_up'], 'B_w_down': _B_in['w_down'], 'c': _w((lambda *s: torch.randn(*s, dtype=torch.bfloat16) * 0.4)(1, 512) * 8.0), 'w_mod': _w((lambda *s: torch.randn(*s, dtype=torch.bfloat16) * 0.4)(512, 6 * 512) * 512 ** (-0.5) * 8.0)}

def reference(A_x, A_k_cache, A_v_cache, A_wq, A_wo, B_x, B_w_gate, B_w_up, B_w_down, c, w_mod):

    def _A_ref(x, k_cache, v_cache, wq, wo):
        B, Sq, D = x.shape
        Hq, Dh = (32, 128)
        Hkv = k_cache.shape[1]
        rep = Hq // Hkv
        q = (lambda x, *d: x.permute(*d))((x @ wq).reshape(B, Sq, Hq, Dh), 0, 2, 1, 3)
        k = (lambda x, n, axis: torch.repeat_interleave(x, n, dim=axis))(k_cache, rep, axis=1)
        v = (lambda x, n, axis: torch.repeat_interleave(x, n, dim=axis))(v_cache, rep, axis=1)
        scores = torch.einsum('bhqd,bhkd->bhqk', q, k) * Dh ** (-0.5)
        p = _softmax(scores, axis=-1)
        o = torch.einsum('bhqk,bhkd->bhqd', p, v)
        return (lambda x, *d: x.permute(*d))(o, 0, 2, 1, 3).reshape(B, Sq, Hq * Dh) @ wo

    def _B_ref(x, w_gate, w_up, w_down):
        g = torch.einsum('bsd,df->bsf', x, w_gate)
        u = torch.einsum('bsd,df->bsf', x, w_up)
        h = _gelu(g) * u
        return torch.einsum('bsf,fd->bsd', h, w_down)
    x = A_x
    mod = (_silu(c) @ w_mod).reshape(c.shape[0], 6, 1, -1)
    sa, ca, ga, sb, cb, gb = (mod[:, 0].clone(), mod[:, 1].clone(), mod[:, 2].clone(), mod[:, 3].clone(), mod[:, 4].clone(), mod[:, 5].clone())
    h = _layernorm(x, axis=-1) * (1 + ca) + sa
    x = x + ga * _A_ref(h, A_k_cache, A_v_cache, A_wq, A_wo)
    h = _layernorm(x, axis=-1) * (1 + cb) + sb
    x = x + gb * _B_ref(h, B_w_gate, B_w_up, B_w_down)
    return x
```

```cpp
#include <hip/hip_runtime.h>
#include <hip/hip_bf16.h>
#include <utility>
#include <type_traits>
#include <stdint.h>

constexpr int SEQ = 2048, DM = 512, NH = 32, DH = 128, DF = 1376;
constexpr int D = 128, NWAVE = 4, QROWS = 64, KVBLK = 64;
#define ATTN_THR 32.0f
constexpr float THR = ATTN_THR;
constexpr int S = SEQ, NT = S / KVBLK;
constexpr unsigned KRS = 256u, QRS = NH * 256u, KTILEB = 64u * KRS;
constexpr size_t SHM_ATTN = 163840;
typedef float    f32x16 __attribute__((ext_vector_type(16)));
typedef float    f32x2  __attribute__((ext_vector_type(2)));
typedef float    f32x4  __attribute__((ext_vector_type(4)));
typedef __bf16   bf16x8 __attribute__((ext_vector_type(8)));
typedef __bf16   bf16x4 __attribute__((ext_vector_type(4)));
typedef __bf16   bf16x2 __attribute__((ext_vector_type(2)));
typedef unsigned u32x4  __attribute__((ext_vector_type(4)));
typedef unsigned u32x2  __attribute__((ext_vector_type(2)));
template <int... Is> using iseq = std::integer_sequence<int, Is...>;
template <int N> using make_iseq = std::make_integer_sequence<int, N>;

#define KRING 0x0000u
#define VRING 0x8000u
#define QSTAGE 0x10000u
#define OSTAGE 0x20000u
#define SRD_WORD3 0x00020000
#define DI __device__ __forceinline__
#define GAP() __builtin_amdgcn_sched_barrier(0)
#define W4_ACLOB_HI "a128","a129","a130","a131","a132","a133","a134","a135","a136","a137","a138","a139","a140","a141","a142","a143","a144","a145","a146","a147","a148","a149","a150","a151","a152","a153","a154","a155","a156","a157","a158","a159","a160","a161","a162","a163","a164","a165","a166","a167","a168","a169","a170","a171","a172","a173","a174","a175","a176","a177","a178","a179","a180","a181","a182","a183","a184","a185","a186","a187","a188","a189","a190","a191","a192","a193","a194","a195","a196","a197","a198","a199","a200","a201","a202","a203","a204","a205","a206","a207","a208","a209","a210","a211","a212","a213","a214","a215","a216","a217","a218","a219","a220","a221","a222","a223","a224","a225","a226","a227","a228","a229","a230","a231","a232","a233","a234","a235","a236","a237","a238","a239","a240","a241","a242","a243","a244","a245","a246","a247","a248","a249","a250","a251","a252","a253","a254","a255"
#define W4_ACLOB "a0","a1","a2","a3","a4","a5","a6","a7","a8","a9","a10","a11","a12","a13","a14","a15","a16","a17","a18","a19","a20","a21","a22","a23","a24","a25","a26","a27","a28","a29","a30","a31","a32","a33","a34","a35","a36","a37","a38","a39","a40","a41","a42","a43","a44","a45","a46","a47","a48","a49","a50","a51","a52","a53","a54","a55","a56","a57","a58","a59","a60","a61","a62","a63","a64","a65","a66","a67","a68","a69","a70","a71","a72","a73","a74","a75","a76","a77","a78","a79","a80","a81","a82","a83","a84","a85","a86","a87","a88","a89","a90","a91","a92","a93","a94","a95","a96","a97","a98","a99","a100","a101","a102","a103","a104","a105","a106","a107","a108","a109","a110","a111","a112","a113","a114","a115","a116","a117","a118","a119","a120","a121","a122","a123","a124","a125","a126","a127"
#define W4_READ_O(DB, QB, o) do { switch (2 * (DB) + (QB)) { \
    case 0: asm volatile("v_accvgpr_read_b32 %0, a0\n\t" "v_accvgpr_read_b32 %1, a1\n\t" "v_accvgpr_read_b32 %2, a2\n\t" "v_accvgpr_read_b32 %3, a3\n\t" "v_accvgpr_read_b32 %4, a4\n\t" "v_accvgpr_read_b32 %5, a5\n\t" "v_accvgpr_read_b32 %6, a6\n\t" "v_accvgpr_read_b32 %7, a7\n\t" "v_accvgpr_read_b32 %8, a8\n\t" "v_accvgpr_read_b32 %9, a9\n\t" "v_accvgpr_read_b32 %10, a10\n\t" "v_accvgpr_read_b32 %11, a11\n\t" "v_accvgpr_read_b32 %12, a12\n\t" "v_accvgpr_read_b32 %13, a13\n\t" "v_accvgpr_read_b32 %14, a14\n\t" "v_accvgpr_read_b32 %15, a15\n\t" : "=v"((o)[0]), "=v"((o)[1]), "=v"((o)[2]), "=v"((o)[3]), "=v"((o)[4]), "=v"((o)[5]), "=v"((o)[6]), "=v"((o)[7]), "=v"((o)[8]), "=v"((o)[9]), "=v"((o)[10]), "=v"((o)[11]), "=v"((o)[12]), "=v"((o)[13]), "=v"((o)[14]), "=v"((o)[15])); break; \
    case 1: asm volatile("v_accvgpr_read_b32 %0, a16\n\t" "v_accvgpr_read_b32 %1, a17\n\t" "v_accvgpr_read_b32 %2, a18\n\t" "v_accvgpr_read_b32 %3, a19\n\t" "v_accvgpr_read_b32 %4, a20\n\t" "v_accvgpr_read_b32 %5, a21\n\t" "v_accvgpr_read_b32 %6, a22\n\t" "v_accvgpr_read_b32 %7, a23\n\t" "v_accvgpr_read_b32 %8, a24\n\t" "v_accvgpr_read_b32 %9, a25\n\t" "v_accvgpr_read_b32 %10, a26\n\t" "v_accvgpr_read_b32 %11, a27\n\t" "v_accvgpr_read_b32 %12, a28\n\t" "v_accvgpr_read_b32 %13, a29\n\t" "v_accvgpr_read_b32 %14, a30\n\t" "v_accvgpr_read_b32 %15, a31\n\t" : "=v"((o)[0]), "=v"((o)[1]), "=v"((o)[2]), "=v"((o)[3]), "=v"((o)[4]), "=v"((o)[5]), "=v"((o)[6]), "=v"((o)[7]), "=v"((o)[8]), "=v"((o)[9]), "=v"((o)[10]), "=v"((o)[11]), "=v"((o)[12]), "=v"((o)[13]), "=v"((o)[14]), "=v"((o)[15])); break; \
    case 2: asm volatile("v_accvgpr_read_b32 %0, a32\n\t" "v_accvgpr_read_b32 %1, a33\n\t" "v_accvgpr_read_b32 %2, a34\n\t" "v_accvgpr_read_b32 %3, a35\n\t" "v_accvgpr_read_b32 %4, a36\n\t" "v_accvgpr_read_b32 %5, a37\n\t" "v_accvgpr_read_b32 %6, a38\n\t" "v_accvgpr_read_b32 %7, a39\n\t" "v_accvgpr_read_b32 %8, a40\n\t" "v_accvgpr_read_b32 %9, a41\n\t" "v_accvgpr_read_b32 %10, a42\n\t" "v_accvgpr_read_b32 %11, a43\n\t" "v_accvgpr_read_b32 %12, a44\n\t" "v_accvgpr_read_b32 %13, a45\n\t" "v_accvgpr_read_b32 %14, a46\n\t" "v_accvgpr_read_b32 %15, a47\n\t" : "=v"((o)[0]), "=v"((o)[1]), "=v"((o)[2]), "=v"((o)[3]), "=v"((o)[4]), "=v"((o)[5]), "=v"((o)[6]), "=v"((o)[7]), "=v"((o)[8]), "=v"((o)[9]), "=v"((o)[10]), "=v"((o)[11]), "=v"((o)[12]), "=v"((o)[13]), "=v"((o)[14]), "=v"((o)[15])); break; \
    case 3: asm volatile("v_accvgpr_read_b32 %0, a48\n\t" "v_accvgpr_read_b32 %1, a49\n\t" "v_accvgpr_read_b32 %2, a50\n\t" "v_accvgpr_read_b32 %3, a51\n\t" "v_accvgpr_read_b32 %4, a52\n\t" "v_accvgpr_read_b32 %5, a53\n\t" "v_accvgpr_read_b32 %6, a54\n\t" "v_accvgpr_read_b32 %7, a55\n\t" "v_accvgpr_read_b32 %8, a56\n\t" "v_accvgpr_read_b32 %9, a57\n\t" "v_accvgpr_read_b32 %10, a58\n\t" "v_accvgpr_read_b32 %11, a59\n\t" "v_accvgpr_read_b32 %12, a60\n\t" "v_accvgpr_read_b32 %13, a61\n\t" "v_accvgpr_read_b32 %14, a62\n\t" "v_accvgpr_read_b32 %15, a63\n\t" : "=v"((o)[0]), "=v"((o)[1]), "=v"((o)[2]), "=v"((o)[3]), "=v"((o)[4]), "=v"((o)[5]), "=v"((o)[6]), "=v"((o)[7]), "=v"((o)[8]), "=v"((o)[9]), "=v"((o)[10]), "=v"((o)[11]), "=v"((o)[12]), "=v"((o)[13]), "=v"((o)[14]), "=v"((o)[15])); break; \
    case 4: asm volatile("v_accvgpr_read_b32 %0, a64\n\t" "v_accvgpr_read_b32 %1, a65\n\t" "v_accvgpr_read_b32 %2, a66\n\t" "v_accvgpr_read_b32 %3, a67\n\t" "v_accvgpr_read_b32 %4, a68\n\t" "v_accvgpr_read_b32 %5, a69\n\t" "v_accvgpr_read_b32 %6, a70\n\t" "v_accvgpr_read_b32 %7, a71\n\t" "v_accvgpr_read_b32 %8, a72\n\t" "v_accvgpr_read_b32 %9, a73\n\t" "v_accvgpr_read_b32 %10, a74\n\t" "v_accvgpr_read_b32 %11, a75\n\t" "v_accvgpr_read_b32 %12, a76\n\t" "v_accvgpr_read_b32 %13, a77\n\t" "v_accvgpr_read_b32 %14, a78\n\t" "v_accvgpr_read_b32 %15, a79\n\t" : "=v"((o)[0]), "=v"((o)[1]), "=v"((o)[2]), "=v"((o)[3]), "=v"((o)[4]), "=v"((o)[5]), "=v"((o)[6]), "=v"((o)[7]), "=v"((o)[8]), "=v"((o)[9]), "=v"((o)[10]), "=v"((o)[11]), "=v"((o)[12]), "=v"((o)[13]), "=v"((o)[14]), "=v"((o)[15])); break; \
    case 5: asm volatile("v_accvgpr_read_b32 %0, a80\n\t" "v_accvgpr_read_b32 %1, a81\n\t" "v_accvgpr_read_b32 %2, a82\n\t" "v_accvgpr_read_b32 %3, a83\n\t" "v_accvgpr_read_b32 %4, a84\n\t" "v_accvgpr_read_b32 %5, a85\n\t" "v_accvgpr_read_b32 %6, a86\n\t" "v_accvgpr_read_b32 %7, a87\n\t" "v_accvgpr_read_b32 %8, a88\n\t" "v_accvgpr_read_b32 %9, a89\n\t" "v_accvgpr_read_b32 %10, a90\n\t" "v_accvgpr_read_b32 %11, a91\n\t" "v_accvgpr_read_b32 %12, a92\n\t" "v_accvgpr_read_b32 %13, a93\n\t" "v_accvgpr_read_b32 %14, a94\n\t" "v_accvgpr_read_b32 %15, a95\n\t" : "=v"((o)[0]), "=v"((o)[1]), "=v"((o)[2]), "=v"((o)[3]), "=v"((o)[4]), "=v"((o)[5]), "=v"((o)[6]), "=v"((o)[7]), "=v"((o)[8]), "=v"((o)[9]), "=v"((o)[10]), "=v"((o)[11]), "=v"((o)[12]), "=v"((o)[13]), "=v"((o)[14]), "=v"((o)[15])); break; \
    case 6: asm volatile("v_accvgpr_read_b32 %0, a96\n\t" "v_accvgpr_read_b32 %1, a97\n\t" "v_accvgpr_read_b32 %2, a98\n\t" "v_accvgpr_read_b32 %3, a99\n\t" "v_accvgpr_read_b32 %4, a100\n\t" "v_accvgpr_read_b32 %5, a101\n\t" "v_accvgpr_read_b32 %6, a102\n\t" "v_accvgpr_read_b32 %7, a103\n\t" "v_accvgpr_read_b32 %8, a104\n\t" "v_accvgpr_read_b32 %9, a105\n\t" "v_accvgpr_read_b32 %10, a106\n\t" "v_accvgpr_read_b32 %11, a107\n\t" "v_accvgpr_read_b32 %12, a108\n\t" "v_accvgpr_read_b32 %13, a109\n\t" "v_accvgpr_read_b32 %14, a110\n\t" "v_accvgpr_read_b32 %15, a111\n\t" : "=v"((o)[0]), "=v"((o)[1]), "=v"((o)[2]), "=v"((o)[3]), "=v"((o)[4]), "=v"((o)[5]), "=v"((o)[6]), "=v"((o)[7]), "=v"((o)[8]), "=v"((o)[9]), "=v"((o)[10]), "=v"((o)[11]), "=v"((o)[12]), "=v"((o)[13]), "=v"((o)[14]), "=v"((o)[15])); break; \
    case 7: asm volatile("v_accvgpr_read_b32 %0, a112\n\t" "v_accvgpr_read_b32 %1, a113\n\t" "v_accvgpr_read_b32 %2, a114\n\t" "v_accvgpr_read_b32 %3, a115\n\t" "v_accvgpr_read_b32 %4, a116\n\t" "v_accvgpr_read_b32 %5, a117\n\t" "v_accvgpr_read_b32 %6, a118\n\t" "v_accvgpr_read_b32 %7, a119\n\t" "v_accvgpr_read_b32 %8, a120\n\t" "v_accvgpr_read_b32 %9, a121\n\t" "v_accvgpr_read_b32 %10, a122\n\t" "v_accvgpr_read_b32 %11, a123\n\t" "v_accvgpr_read_b32 %12, a124\n\t" "v_accvgpr_read_b32 %13, a125\n\t" "v_accvgpr_read_b32 %14, a126\n\t" "v_accvgpr_read_b32 %15, a127\n\t" : "=v"((o)[0]), "=v"((o)[1]), "=v"((o)[2]), "=v"((o)[3]), "=v"((o)[4]), "=v"((o)[5]), "=v"((o)[6]), "=v"((o)[7]), "=v"((o)[8]), "=v"((o)[9]), "=v"((o)[10]), "=v"((o)[11]), "=v"((o)[12]), "=v"((o)[13]), "=v"((o)[14]), "=v"((o)[15])); break; \
    } } while (0)

DI unsigned rfl(unsigned x) { return __builtin_amdgcn_readfirstlane(x); }
DI unsigned cvtpk(float lo, float hi) {
  f32x2 v = {lo, hi}; bf16x2 b = __builtin_convertvector(v, bf16x2); return __builtin_bit_cast(unsigned, b);
}
struct W4C {
  unsigned vkb[4], vvb, vksrc[2], vvsrc, m0k, m0v, sofk, sofv, ldsb;
  __amdgpu_buffer_rsrc_t srdK, srdV;
  float lsum[2];
};
constexpr int AO_(int db, int qb) { return 16 * (2 * db + qb); }
constexpr int AQ_(int qb, int d0) { return 128 + 32 * qb + 4 * d0; }
constexpr int AK_(int kb, int d0) { return 192 + 32 * kb + 4 * d0; }
template <int I> DI void k_read(W4C& c, unsigned slot) {
  constexpr int kb = I >> 3, d0 = I & 7;
  asm volatile("ds_read_b128 a[%c1:%c2], %0 offset:%c3" :: "v"(c.vkb[d0 & 3] + slot), "i"(AK_(kb, d0)), "i"(AK_(kb, d0) + 3), "i"(8192 * kb + 128 * (d0 >> 2)) : "memory", W4_ACLOB_HI);
}
template <int I> DI void q_write1(const u32x4& w) {
  constexpr int aq = AQ_(I >> 3, I & 7);
  asm volatile("v_accvgpr_write_b32 a[%c4], %0\n\tv_accvgpr_write_b32 a[%c5], %1\n\tv_accvgpr_write_b32 a[%c6], %2\n\tv_accvgpr_write_b32 a[%c7], %3"
               :: "v"(w[0]), "v"(w[1]), "v"(w[2]), "v"(w[3]), "i"(aq), "i"(aq + 1), "i"(aq + 2), "i"(aq + 3) : W4_ACLOB_HI);
}
DI void q_write(const int qb, const int d0, const u32x4& w) {
  switch (8 * qb + d0) {
    case 0: q_write1<0>(w); break;   case 1: q_write1<1>(w); break;   case 2: q_write1<2>(w); break;   case 3: q_write1<3>(w); break;
    case 4: q_write1<4>(w); break;   case 5: q_write1<5>(w); break;   case 6: q_write1<6>(w); break;   case 7: q_write1<7>(w); break;
    case 8: q_write1<8>(w); break;   case 9: q_write1<9>(w); break;   case 10: q_write1<10>(w); break; case 11: q_write1<11>(w); break;
    case 12: q_write1<12>(w); break; case 13: q_write1<13>(w); break; case 14: q_write1<14>(w); break; case 15: q_write1<15>(w); break;
  }
}
template <int... Is> DI void k_reads(W4C& c, unsigned slot, iseq<Is...>) { (k_read<Is>(c, slot), ...); }
#define ATTN_CORE_ASM \
  "v_mfma_f32_32x32x16_bf16 v[0:15], a[192:195], a[128:131], 0\n\t" \
  "v_accvgpr_write_b32 a0, 0\n\t" \
  "v_accvgpr_write_b32 a1, 0\n\t" \
  "v_accvgpr_write_b32 a2, 0\n\t" \
  "v_accvgpr_write_b32 a3, 0\n\t" \
  "v_mfma_f32_32x32x16_bf16 v[16:31], a[192:195], a[160:163], 0\n\t" \
  "v_accvgpr_write_b32 a4, 0\n\t" \
  "v_accvgpr_write_b32 a5, 0\n\t" \
  "v_accvgpr_write_b32 a6, 0\n\t" \
  "v_accvgpr_write_b32 a7, 0\n\t" \
  "v_mfma_f32_32x32x16_bf16 v[32:47], a[224:227], a[128:131], 0\n\t" \
  "v_accvgpr_write_b32 a8, 0\n\t" \
  "v_accvgpr_write_b32 a9, 0\n\t" \
  "v_accvgpr_write_b32 a10, 0\n\t" \
  "v_accvgpr_write_b32 a11, 0\n\t" \
  "v_mfma_f32_32x32x16_bf16 v[48:63], a[224:227], a[160:163], 0\n\t" \
  "v_accvgpr_write_b32 a12, 0\n\t" \
  "v_accvgpr_write_b32 a13, 0\n\t" \
  "v_accvgpr_write_b32 a14, 0\n\t" \
  "v_accvgpr_write_b32 a15, 0\n\t" \
  "v_mfma_f32_32x32x16_bf16 v[0:15], a[196:199], a[132:135], v[0:15]\n\t" \
  "v_accvgpr_write_b32 a16, 0\n\t" \
  "v_accvgpr_write_b32 a17, 0\n\t" \
  "v_accvgpr_write_b32 a18, 0\n\t" \
  "v_accvgpr_write_b32 a19, 0\n\t" \
  "v_mfma_f32_32x32x16_bf16 v[16:31], a[196:199], a[164:167], v[16:31]\n\t" \
  "v_accvgpr_write_b32 a20, 0\n\t" \
  "v_accvgpr_write_b32 a21, 0\n\t" \
  "v_accvgpr_write_b32 a22, 0\n\t" \
  "v_accvgpr_write_b32 a23, 0\n\t" \
  "v_mfma_f32_32x32x16_bf16 v[32:47], a[228:231], a[132:135], v[32:47]\n\t" \
  "v_accvgpr_write_b32 a24, 0\n\t" \
  "v_accvgpr_write_b32 a25, 0\n\t" \
  "v_accvgpr_write_b32 a26, 0\n\t" \
  "v_accvgpr_write_b32 a27, 0\n\t" \
  "v_mfma_f32_32x32x16_bf16 v[48:63], a[228:231], a[164:167], v[48:63]\n\t" \
  "v_accvgpr_write_b32 a28, 0\n\t" \
  "v_accvgpr_write_b32 a29, 0\n\t" \
  "v_accvgpr_write_b32 a30, 0\n\t" \
  "v_accvgpr_write_b32 a31, 0\n\t" \
  "v_mfma_f32_32x32x16_bf16 v[0:15], a[200:203], a[136:139], v[0:15]\n\t" \
  "v_accvgpr_write_b32 a32, 0\n\t" \
  "v_accvgpr_write_b32 a33, 0\n\t" \
  "v_accvgpr_write_b32 a34, 0\n\t" \
  "v_accvgpr_write_b32 a35, 0\n\t" \
  "v_mfma_f32_32x32x16_bf16 v[16:31], a[200:203], a[168:171], v[16:31]\n\t" \
  "v_accvgpr_write_b32 a36, 0\n\t" \
  "v_accvgpr_write_b32 a37, 0\n\t" \
  "v_accvgpr_write_b32 a38, 0\n\t" \
  "v_accvgpr_write_b32 a39, 0\n\t" \
  "v_mfma_f32_32x32x16_bf16 v[32:47], a[232:235], a[136:139], v[32:47]\n\t" \
  "v_accvgpr_write_b32 a40, 0\n\t" \
  "v_accvgpr_write_b32 a41, 0\n\t" \
  "v_accvgpr_write_b32 a42, 0\n\t" \
  "v_accvgpr_write_b32 a43, 0\n\t" \
  "v_mfma_f32_32x32x16_bf16 v[48:63], a[232:235], a[168:171], v[48:63]\n\t" \
  "v_accvgpr_write_b32 a44, 0\n\t" \
  "v_accvgpr_write_b32 a45, 0\n\t" \
  "v_accvgpr_write_b32 a46, 0\n\t" \
  "v_accvgpr_write_b32 a47, 0\n\t" \
  "v_mfma_f32_32x32x16_bf16 v[0:15], a[204:207], a[140:143], v[0:15]\n\t" \
  "v_accvgpr_write_b32 a48, 0\n\t" \
  "v_accvgpr_write_b32 a49, 0\n\t" \
  "v_accvgpr_write_b32 a50, 0\n\t" \
  "v_accvgpr_write_b32 a51, 0\n\t" \
  "v_mfma_f32_32x32x16_bf16 v[16:31], a[204:207], a[172:175], v[16:31]\n\t" \
  "v_accvgpr_write_b32 a52, 0\n\t" \
  "v_accvgpr_write_b32 a53, 0\n\t" \
  "v_accvgpr_write_b32 a54, 0\n\t" \
  "v_accvgpr_write_b32 a55, 0\n\t" \
  "v_mfma_f32_32x32x16_bf16 v[32:47], a[236:239], a[140:143], v[32:47]\n\t" \
  "v_accvgpr_write_b32 a56, 0\n\t" \
  "v_accvgpr_write_b32 a57, 0\n\t" \
  "v_accvgpr_write_b32 a58, 0\n\t" \
  "v_accvgpr_write_b32 a59, 0\n\t" \
  "v_mfma_f32_32x32x16_bf16 v[48:63], a[236:239], a[172:175], v[48:63]\n\t" \
  "v_accvgpr_write_b32 a60, 0\n\t" \
  "v_accvgpr_write_b32 a61, 0\n\t" \
  "v_accvgpr_write_b32 a62, 0\n\t" \
  "v_accvgpr_write_b32 a63, 0\n\t" \
  "v_mfma_f32_32x32x16_bf16 v[0:15], a[208:211], a[144:147], v[0:15]\n\t" \
  "v_accvgpr_write_b32 a64, 0\n\t" \
  "v_accvgpr_write_b32 a65, 0\n\t" \
  "v_accvgpr_write_b32 a66, 0\n\t" \
  "v_accvgpr_write_b32 a67, 0\n\t" \
  "v_mfma_f32_32x32x16_bf16 v[16:31], a[208:211], a[176:179], v[16:31]\n\t" \
  "v_accvgpr_write_b32 a68, 0\n\t" \
  "v_accvgpr_write_b32 a69, 0\n\t" \
  "v_accvgpr_write_b32 a70, 0\n\t" \
  "v_accvgpr_write_b32 a71, 0\n\t" \
  "v_mfma_f32_32x32x16_bf16 v[32:47], a[240:243], a[144:147], v[32:47]\n\t" \
  "v_accvgpr_write_b32 a72, 0\n\t" \
  "v_accvgpr_write_b32 a73, 0\n\t" \
  "v_accvgpr_write_b32 a74, 0\n\t" \
  "v_accvgpr_write_b32 a75, 0\n\t" \
  "v_mfma_f32_32x32x16_bf16 v[48:63], a[240:243], a[176:179], v[48:63]\n\t" \
  "v_accvgpr_write_b32 a76, 0\n\t" \
  "v_accvgpr_write_b32 a77, 0\n\t" \
  "v_accvgpr_write_b32 a78, 0\n\t" \
  "v_accvgpr_write_b32 a79, 0\n\t" \
  "v_mfma_f32_32x32x16_bf16 v[0:15], a[212:215], a[148:151], v[0:15]\n\t" \
  "v_accvgpr_write_b32 a80, 0\n\t" \
  "v_accvgpr_write_b32 a81, 0\n\t" \
  "v_accvgpr_write_b32 a82, 0\n\t" \
  "v_accvgpr_write_b32 a83, 0\n\t" \
  "v_mfma_f32_32x32x16_bf16 v[16:31], a[212:215], a[180:183], v[16:31]\n\t" \
  "v_accvgpr_write_b32 a84, 0\n\t" \
  "v_accvgpr_write_b32 a85, 0\n\t" \
  "v_accvgpr_write_b32 a86, 0\n\t" \
  "v_accvgpr_write_b32 a87, 0\n\t" \
  "v_mfma_f32_32x32x16_bf16 v[32:47], a[244:247], a[148:151], v[32:47]\n\t" \
  "v_accvgpr_write_b32 a88, 0\n\t" \
  "v_accvgpr_write_b32 a89, 0\n\t" \
  "v_accvgpr_write_b32 a90, 0\n\t" \
  "v_accvgpr_write_b32 a91, 0\n\t" \
  "v_mfma_f32_32x32x16_bf16 v[48:63], a[244:247], a[180:183], v[48:63]\n\t" \
  "v_accvgpr_write_b32 a92, 0\n\t" \
  "v_accvgpr_write_b32 a93, 0\n\t" \
  "v_accvgpr_write_b32 a94, 0\n\t" \
  "v_accvgpr_write_b32 a95, 0\n\t" \
  "v_mfma_f32_32x32x16_bf16 v[0:15], a[216:219], a[152:155], v[0:15]\n\t" \
  "v_accvgpr_write_b32 a96, 0\n\t" \
  "v_accvgpr_write_b32 a97, 0\n\t" \
  "v_accvgpr_write_b32 a98, 0\n\t" \
  "v_accvgpr_write_b32 a99, 0\n\t" \
  "v_mfma_f32_32x32x16_bf16 v[16:31], a[216:219], a[184:187], v[16:31]\n\t" \
  "v_accvgpr_write_b32 a100, 0\n\t" \
  "v_accvgpr_write_b32 a101, 0\n\t" \
  "v_accvgpr_write_b32 a102, 0\n\t" \
  "v_accvgpr_write_b32 a103, 0\n\t" \
  "v_mfma_f32_32x32x16_bf16 v[32:47], a[248:251], a[152:155], v[32:47]\n\t" \
  "v_accvgpr_write_b32 a104, 0\n\t" \
  "v_accvgpr_write_b32 a105, 0\n\t" \
  "v_accvgpr_write_b32 a106, 0\n\t" \
  "v_accvgpr_write_b32 a107, 0\n\t" \
  "v_mfma_f32_32x32x16_bf16 v[48:63], a[248:251], a[184:187], v[48:63]\n\t" \
  "v_accvgpr_write_b32 a108, 0\n\t" \
  "v_accvgpr_write_b32 a109, 0\n\t" \
  "v_accvgpr_write_b32 a110, 0\n\t" \
  "v_accvgpr_write_b32 a111, 0\n\t" \
  "v_mfma_f32_32x32x16_bf16 v[0:15], a[220:223], a[156:159], v[0:15]\n\t" \
  "v_accvgpr_write_b32 a112, 0\n\t" \
  "v_accvgpr_write_b32 a113, 0\n\t" \
  "v_accvgpr_write_b32 a114, 0\n\t" \
  "v_accvgpr_write_b32 a115, 0\n\t" \
  "v_mfma_f32_32x32x16_bf16 v[16:31], a[220:223], a[188:191], v[16:31]\n\t" \
  "v_accvgpr_write_b32 a116, 0\n\t" \
  "v_accvgpr_write_b32 a117, 0\n\t" \
  "v_accvgpr_write_b32 a118, 0\n\t" \
  "v_accvgpr_write_b32 a119, 0\n\t" \
  "v_mfma_f32_32x32x16_bf16 v[32:47], a[252:255], a[156:159], v[32:47]\n\t" \
  "v_accvgpr_write_b32 a120, 0\n\t" \
  "v_accvgpr_write_b32 a121, 0\n\t" \
  "v_accvgpr_write_b32 a122, 0\n\t" \
  "v_accvgpr_write_b32 a123, 0\n\t" \
  "v_mfma_f32_32x32x16_bf16 v[48:63], a[252:255], a[188:191], v[48:63]\n\t" \
  "v_accvgpr_write_b32 a124, 0\n\t" \
  "v_accvgpr_write_b32 a125, 0\n\t" \
  "v_accvgpr_write_b32 a126, 0\n\t" \
  "v_accvgpr_write_b32 a127, 0\n\t" \
  "s_waitcnt vmcnt(0) lgkmcnt(0)\n\t" \
  "s_barrier\n\t" \
  "s_nop 7\n\t" \
  "s_nop 7\n\t" \
  "v_max3_f32 v240, v0, v1, v2\n\t" \
  "v_max3_f32 v242, v16, v17, v18\n\t" \
  "v_max3_f32 v241, v32, v33, v34\n\t" \
  "v_max3_f32 v243, v48, v49, v50\n\t" \
  "s_mov_b32 m0, %[m0k0]\n\t" \
  "s_nop 0\n\t" \
  "v_max3_f32 v240, v240, v3, v4\n\t" \
  "v_max3_f32 v242, v242, v19, v20\n\t" \
  "v_max3_f32 v241, v241, v35, v36\n\t" \
  "v_max3_f32 v243, v243, v51, v52\n\t" \
  "buffer_load_dwordx4 v229, %[srdk], %[sok] offen offset:0 lds\n\t" \
  "buffer_load_dwordx4 v230, %[srdk], %[sok] offen offset:1024 lds\n\t" \
  "v_max3_f32 v240, v240, v5, v6\n\t" \
  "v_max3_f32 v242, v242, v21, v22\n\t" \
  "v_max3_f32 v241, v241, v37, v38\n\t" \
  "v_max3_f32 v243, v243, v53, v54\n\t" \
  "buffer_load_dwordx4 v229, %[srdk], %[sok] offen offset:2048 lds\n\t" \
  "buffer_load_dwordx4 v230, %[srdk], %[sok] offen offset:3072 lds\n\t" \
  "v_max3_f32 v240, v240, v7, v8\n\t" \
  "v_max3_f32 v242, v242, v23, v24\n\t" \
  "v_max3_f32 v241, v241, v39, v40\n\t" \
  "v_max3_f32 v243, v243, v55, v56\n\t" \
  "s_mov_b32 m0, %[m0v1]\n\t" \
  "s_nop 0\n\t" \
  "v_max3_f32 v240, v240, v9, v10\n\t" \
  "v_max3_f32 v242, v242, v25, v26\n\t" \
  "v_max3_f32 v241, v241, v41, v42\n\t" \
  "v_max3_f32 v243, v243, v57, v58\n\t" \
  "buffer_load_dwordx4 v231, %[srdv], %[sov] offen offset:0 lds\n\t" \
  "buffer_load_dwordx4 v231, %[srdv], %[sov] offen offset:2048 lds\n\t" \
  "v_max3_f32 v240, v240, v11, v12\n\t" \
  "v_max3_f32 v242, v242, v27, v28\n\t" \
  "v_max3_f32 v241, v241, v43, v44\n\t" \
  "v_max3_f32 v243, v243, v59, v60\n\t" \
  "s_add_u32 m0, %[m0v1], 1024\n\t" \
  "s_nop 0\n\t" \
  "v_max3_f32 v240, v240, v13, v14\n\t" \
  "v_max3_f32 v242, v242, v29, v30\n\t" \
  "v_max3_f32 v241, v241, v45, v46\n\t" \
  "v_max3_f32 v243, v243, v61, v62\n\t" \
  "buffer_load_dwordx4 v231, %[srdv], %[sov2] offen offset:0 lds\n\t" \
  "buffer_load_dwordx4 v231, %[srdv], %[sov2] offen offset:2048 lds\n\t" \
  "v_max_f32 v240, v240, v15\n\t" \
  "v_max_f32 v242, v242, v31\n\t" \
  "v_max_f32 v241, v241, v47\n\t" \
  "v_max_f32 v243, v243, v63\n\t" \
  "s_add_u32 %[sok], %[sok], 16384\n\t" \
  "s_add_u32 %[sov], %[sov], 16384\n\t" \
  "s_add_u32 %[sov2], %[sov2], 16384\n\t" \
  "v_max_f32 v248, v240, v241\n\t" \
  "v_max_f32 v249, v242, v243\n\t" \
  "v_mov_b32 v250, v248\n\t" \
  "v_mov_b32 v251, v249\n\t" \
  "s_nop 1\n\t" \
  "v_permlane32_swap_b32 v248, v250\n\t" \
  "v_permlane32_swap_b32 v249, v251\n\t" \
  "v_max_f32 v244, v248, v250\n\t" \
  "v_max_f32 v245, v249, v251\n\t" \
  "v_mov_b32 v236, v244\n\t" \
  "v_sub_f32 v0, v0, v244\n\t" \
  "v_sub_f32 v1, v1, v244\n\t" \
  "v_sub_f32 v2, v2, v244\n\t" \
  "v_sub_f32 v3, v3, v244\n\t" \
  "v_sub_f32 v4, v4, v244\n\t" \
  "v_sub_f32 v5, v5, v244\n\t" \
  "v_sub_f32 v6, v6, v244\n\t" \
  "v_sub_f32 v7, v7, v244\n\t" \
  "v_sub_f32 v8, v8, v244\n\t" \
  "v_sub_f32 v9, v9, v244\n\t" \
  "v_sub_f32 v10, v10, v244\n\t" \
  "v_sub_f32 v11, v11, v244\n\t" \
  "v_sub_f32 v12, v12, v244\n\t" \
  "v_sub_f32 v13, v13, v244\n\t" \
  "v_sub_f32 v14, v14, v244\n\t" \
  "v_sub_f32 v15, v15, v244\n\t" \
  "v_sub_f32 v32, v32, v244\n\t" \
  "v_sub_f32 v33, v33, v244\n\t" \
  "v_sub_f32 v34, v34, v244\n\t" \
  "v_sub_f32 v35, v35, v244\n\t" \
  "v_sub_f32 v36, v36, v244\n\t" \
  "v_sub_f32 v37, v37, v244\n\t" \
  "v_sub_f32 v38, v38, v244\n\t" \
  "v_sub_f32 v39, v39, v244\n\t" \
  "v_sub_f32 v40, v40, v244\n\t" \
  "v_sub_f32 v41, v41, v244\n\t" \
  "v_sub_f32 v42, v42, v244\n\t" \
  "v_sub_f32 v43, v43, v244\n\t" \
  "v_sub_f32 v44, v44, v244\n\t" \
  "v_sub_f32 v45, v45, v244\n\t" \
  "v_sub_f32 v46, v46, v244\n\t" \
  "v_sub_f32 v47, v47, v244\n\t" \
  "v_sub_f32 v192, 0, v244\n\t" \
  "v_sub_f32 v193, 0, v244\n\t" \
  "v_sub_f32 v194, 0, v244\n\t" \
  "v_sub_f32 v195, 0, v244\n\t" \
  "v_sub_f32 v196, 0, v244\n\t" \
  "v_sub_f32 v197, 0, v244\n\t" \
  "v_sub_f32 v198, 0, v244\n\t" \
  "v_sub_f32 v199, 0, v244\n\t" \
  "v_sub_f32 v200, 0, v244\n\t" \
  "v_sub_f32 v201, 0, v244\n\t" \
  "v_sub_f32 v202, 0, v244\n\t" \
  "v_sub_f32 v203, 0, v244\n\t" \
  "v_sub_f32 v204, 0, v244\n\t" \
  "v_sub_f32 v205, 0, v244\n\t" \
  "v_sub_f32 v206, 0, v244\n\t" \
  "v_sub_f32 v207, 0, v244\n\t" \
  "v_mov_b32 v237, v245\n\t" \
  "v_sub_f32 v16, v16, v245\n\t" \
  "v_sub_f32 v17, v17, v245\n\t" \
  "v_sub_f32 v18, v18, v245\n\t" \
  "v_sub_f32 v19, v19, v245\n\t" \
  "v_sub_f32 v20, v20, v245\n\t" \
  "v_sub_f32 v21, v21, v245\n\t" \
  "v_sub_f32 v22, v22, v245\n\t" \
  "v_sub_f32 v23, v23, v245\n\t" \
  "v_sub_f32 v24, v24, v245\n\t" \
  "v_sub_f32 v25, v25, v245\n\t" \
  "v_sub_f32 v26, v26, v245\n\t" \
  "v_sub_f32 v27, v27, v245\n\t" \
  "v_sub_f32 v28, v28, v245\n\t" \
  "v_sub_f32 v29, v29, v245\n\t" \
  "v_sub_f32 v30, v30, v245\n\t" \
  "v_sub_f32 v31, v31, v245\n\t" \
  "v_sub_f32 v48, v48, v245\n\t" \
  "v_sub_f32 v49, v49, v245\n\t" \
  "v_sub_f32 v50, v50, v245\n\t" \
  "v_sub_f32 v51, v51, v245\n\t" \
  "v_sub_f32 v52, v52, v245\n\t" \
  "v_sub_f32 v53, v53, v245\n\t" \
  "v_sub_f32 v54, v54, v245\n\t" \
  "v_sub_f32 v55, v55, v245\n\t" \
  "v_sub_f32 v56, v56, v245\n\t" \
  "v_sub_f32 v57, v57, v245\n\t" \
  "v_sub_f32 v58, v58, v245\n\t" \
  "v_sub_f32 v59, v59, v245\n\t" \
  "v_sub_f32 v60, v60, v245\n\t" \
  "v_sub_f32 v61, v61, v245\n\t" \
  "v_sub_f32 v62, v62, v245\n\t" \
  "v_sub_f32 v63, v63, v245\n\t" \
  "v_sub_f32 v208, 0, v245\n\t" \
  "v_sub_f32 v209, 0, v245\n\t" \
  "v_sub_f32 v210, 0, v245\n\t" \
  "v_sub_f32 v211, 0, v245\n\t" \
  "v_sub_f32 v212, 0, v245\n\t" \
  "v_sub_f32 v213, 0, v245\n\t" \
  "v_sub_f32 v214, 0, v245\n\t" \
  "v_sub_f32 v215, 0, v245\n\t" \
  "v_sub_f32 v216, 0, v245\n\t" \
  "v_sub_f32 v217, 0, v245\n\t" \
  "v_sub_f32 v218, 0, v245\n\t" \
  "v_sub_f32 v219, 0, v245\n\t" \
  "v_sub_f32 v220, 0, v245\n\t" \
  "v_sub_f32 v221, 0, v245\n\t" \
  "v_sub_f32 v222, 0, v245\n\t" \
  "v_sub_f32 v223, 0, v245\n\t" \
  "ds_read_b128 a[192:195], v224 offset:16384\n\t" \
  "v_exp_f32 v0, v0\n\t" \
  "v_exp_f32 v1, v1\n\t" \
  "ds_read_b128 a[196:199], v225 offset:16384\n\t" \
  "v_add_f32 v232, 0, v0\n\t" \
  "v_exp_f32 v2, v2\n\t" \
  "v_exp_f32 v3, v3\n\t" \
  "ds_read_b128 a[200:203], v226 offset:16384\n\t" \
  "v_add_f32 v233, 0, v1\n\t" \
  "v_exp_f32 v8, v8\n\t" \
  "v_exp_f32 v9, v9\n\t" \
  "ds_read_b128 a[204:207], v227 offset:16384\n\t" \
  "v_add_f32 v232, v232, v2\n\t" \
  "v_exp_f32 v10, v10\n\t" \
  "v_exp_f32 v11, v11\n\t" \
  "ds_read_b128 a[208:211], v224 offset:16512\n\t" \
  "v_add_f32 v233, v233, v3\n\t" \
  "v_exp_f32 v16, v16\n\t" \
  "v_exp_f32 v17, v17\n\t" \
  "ds_read_b128 a[212:215], v225 offset:16512\n\t" \
  "v_add_f32 v232, v232, v8\n\t" \
  "v_exp_f32 v18, v18\n\t" \
  "v_exp_f32 v19, v19\n\t" \
  "ds_read_b128 a[216:219], v226 offset:16512\n\t" \
  "v_add_f32 v233, v233, v9\n\t" \
  "v_exp_f32 v24, v24\n\t" \
  "v_exp_f32 v25, v25\n\t" \
  "ds_read_b128 a[220:223], v227 offset:16512\n\t" \
  "v_add_f32 v232, v232, v10\n\t" \
  "v_exp_f32 v26, v26\n\t" \
  "v_exp_f32 v27, v27\n\t" \
  "ds_read_b128 a[224:227], v224 offset:24576\n\t" \
  "v_add_f32 v233, v233, v11\n\t" \
  "v_exp_f32 v4, v4\n\t" \
  "v_exp_f32 v5, v5\n\t" \
  "ds_read_b128 a[228:231], v225 offset:24576\n\t" \
  "v_add_f32 v234, 0, v16\n\t" \
  "v_exp_f32 v6, v6\n\t" \
  "v_exp_f32 v7, v7\n\t" \
  "ds_read_b128 a[232:235], v226 offset:24576\n\t" \
  "v_add_f32 v235, 0, v17\n\t" \
  "v_exp_f32 v12, v12\n\t" \
  "v_exp_f32 v13, v13\n\t" \
  "ds_read_b128 a[236:239], v227 offset:24576\n\t" \
  "v_add_f32 v234, v234, v18\n\t" \
  "v_exp_f32 v14, v14\n\t" \
  "v_exp_f32 v15, v15\n\t" \
  "ds_read_b128 a[240:243], v224 offset:24704\n\t" \
  "v_add_f32 v235, v235, v19\n\t" \
  "v_exp_f32 v20, v20\n\t" \
  "v_exp_f32 v21, v21\n\t" \
  "ds_read_b128 a[244:247], v225 offset:24704\n\t" \
  "v_add_f32 v234, v234, v24\n\t" \
  "v_exp_f32 v22, v22\n\t" \
  "ds_read_b128 a[248:251], v226 offset:24704\n\t" \
  "v_add_f32 v235, v235, v25\n\t" \
  "v_exp_f32 v23, v23\n\t" \
  "ds_read_b128 a[252:255], v227 offset:24704\n\t" \
  "v_add_f32 v234, v234, v26\n\t" \
  "v_exp_f32 v28, v28\n\t" \
  "v_add_f32 v235, v235, v27\n\t" \
  "v_exp_f32 v29, v29\n\t" \
  "v_exp_f32 v30, v30\n\t" \
  "v_exp_f32 v31, v31\n\t" \
  "s_waitcnt lgkmcnt(0)\n\t" \
  ".LLOOP%=:\n\t" \
  "v_mfma_f32_32x32x16_bf16 v[64:79], a[192:195], a[128:131], v[192:207]\n\t" \
  "ds_read_b64_tr_b16 v[128:129], v228 offset:0\n\t" \
  "v_exp_f32 v32, v32\n\t" \
  "v_exp_f32 v33, v33\n\t" \
  "v_cvt_pk_bf16_f32 v0, v0, v1\n\t" \
  "v_mfma_f32_32x32x16_bf16 v[80:95], a[192:195], a[160:163], v[208:223]\n\t" \
  "ds_read_b64_tr_b16 v[130:131], v228 offset:2048\n\t" \
  "v_exp_f32 v34, v34\n\t" \
  "v_exp_f32 v35, v35\n\t" \
  "v_cvt_pk_bf16_f32 v1, v2, v3\n\t" \
  "v_mfma_f32_32x32x16_bf16 v[96:111], a[224:227], a[128:131], v[192:207]\n\t" \
  "ds_read_b64_tr_b16 v[132:133], v228 offset:512\n\t" \
  "v_exp_f32 v36, v36\n\t" \
  "v_exp_f32 v37, v37\n\t" \
  "v_cvt_pk_bf16_f32 v2, v4, v5\n\t" \
  "v_mfma_f32_32x32x16_bf16 v[112:127], a[224:227], a[160:163], v[208:223]\n\t" \
  "ds_read_b64_tr_b16 v[134:135], v228 offset:2560\n\t" \
  "v_exp_f32 v38, v38\n\t" \
  "v_exp_f32 v39, v39\n\t" \
  "v_cvt_pk_bf16_f32 v3, v6, v7\n\t" \
  "v_mfma_f32_32x32x16_bf16 v[64:79], a[196:199], a[132:135], v[64:79]\n\t" \
  "ds_read_b64_tr_b16 v[136:137], v228 offset:1024\n\t" \
  "v_add_f32 v232, v232, v4\n\t" \
  "v_exp_f32 v40, v40\n\t" \
  "v_cvt_pk_bf16_f32 v8, v8, v9\n\t" \
  "v_add_f32 v232, v232, v32\n\t" \
  "v_mfma_f32_32x32x16_bf16 v[80:95], a[196:199], a[164:167], v[80:95]\n\t" \
  "ds_read_b64_tr_b16 v[138:139], v228 offset:3072\n\t" \
  "v_add_f32 v233, v233, v5\n\t" \
  "v_exp_f32 v41, v41\n\t" \
  "v_cvt_pk_bf16_f32 v9, v10, v11\n\t" \
  "v_add_f32 v233, v233, v33\n\t" \
  "v_mfma_f32_32x32x16_bf16 v[96:111], a[228:231], a[132:135], v[96:111]\n\t" \
  "ds_read_b64_tr_b16 v[140:141], v228 offset:1536\n\t" \
  "v_add_f32 v232, v232, v6\n\t" \
  "v_exp_f32 v42, v42\n\t" \
  "v_cvt_pk_bf16_f32 v10, v12, v13\n\t" \
  "v_add_f32 v232, v232, v34\n\t" \
  "v_mfma_f32_32x32x16_bf16 v[112:127], a[228:231], a[164:167], v[112:127]\n\t" \
  "ds_read_b64_tr_b16 v[142:143], v228 offset:3584\n\t" \
  "v_add_f32 v233, v233, v7\n\t" \
  "v_exp_f32 v43, v43\n\t" \
  "v_cvt_pk_bf16_f32 v11, v14, v15\n\t" \
  "v_add_f32 v233, v233, v35\n\t" \
  "v_mfma_f32_32x32x16_bf16 v[64:79], a[200:203], a[136:139], v[64:79]\n\t" \
  "ds_read_b64_tr_b16 v[144:145], v228 offset:4096\n\t" \
  "v_add_f32 v232, v232, v12\n\t" \
  "v_exp_f32 v44, v44\n\t" \
  "v_cvt_pk_bf16_f32 v16, v16, v17\n\t" \
  "v_add_f32 v232, v232, v36\n\t" \
  "v_mfma_f32_32x32x16_bf16 v[80:95], a[200:203], a[168:171], v[80:95]\n\t" \
  "ds_read_b64_tr_b16 v[146:147], v228 offset:6144\n\t" \
  "v_add_f32 v233, v233, v13\n\t" \
  "v_exp_f32 v45, v45\n\t" \
  "v_cvt_pk_bf16_f32 v17, v18, v19\n\t" \
  "v_add_f32 v233, v233, v37\n\t" \
  "v_mfma_f32_32x32x16_bf16 v[96:111], a[232:235], a[136:139], v[96:111]\n\t" \
  "ds_read_b64_tr_b16 v[148:149], v228 offset:4608\n\t" \
  "v_add_f32 v232, v232, v14\n\t" \
  "v_exp_f32 v46, v46\n\t" \
  "v_cvt_pk_bf16_f32 v18, v20, v21\n\t" \
  "v_add_f32 v232, v232, v38\n\t" \
  "v_mfma_f32_32x32x16_bf16 v[112:127], a[232:235], a[168:171], v[112:127]\n\t" \
  "ds_read_b64_tr_b16 v[150:151], v228 offset:6656\n\t" \
  "v_add_f32 v233, v233, v15\n\t" \
  "v_exp_f32 v47, v47\n\t" \
  "v_cvt_pk_bf16_f32 v19, v22, v23\n\t" \
  "v_add_f32 v233, v233, v39\n\t" \
  "v_mfma_f32_32x32x16_bf16 v[64:79], a[204:207], a[140:143], v[64:79]\n\t" \
  "ds_read_b64_tr_b16 v[152:153], v228 offset:5120\n\t" \
  "v_add_f32 v234, v234, v20\n\t" \
  "v_exp_f32 v48, v48\n\t" \
  "v_cvt_pk_bf16_f32 v24, v24, v25\n\t" \
  "v_add_f32 v232, v232, v40\n\t" \
  "v_mfma_f32_32x32x16_bf16 v[80:95], a[204:207], a[172:175], v[80:95]\n\t" \
  "ds_read_b64_tr_b16 v[154:155], v228 offset:7168\n\t" \
  "v_add_f32 v235, v235, v21\n\t" \
  "v_exp_f32 v49, v49\n\t" \
  "v_cvt_pk_bf16_f32 v25, v26, v27\n\t" \
  "v_add_f32 v233, v233, v41\n\t" \
  "v_mfma_f32_32x32x16_bf16 v[96:111], a[236:239], a[140:143], v[96:111]\n\t" \
  "ds_read_b64_tr_b16 v[156:157], v228 offset:5632\n\t" \
  "v_add_f32 v234, v234, v22\n\t" \
  "v_exp_f32 v50, v50\n\t" \
  "v_cvt_pk_bf16_f32 v26, v28, v29\n\t" \
  "v_add_f32 v232, v232, v42\n\t" \
  "v_mfma_f32_32x32x16_bf16 v[112:127], a[236:239], a[172:175], v[112:127]\n\t" \
  "ds_read_b64_tr_b16 v[158:159], v228 offset:7680\n\t" \
  "v_add_f32 v235, v235, v23\n\t" \
  "v_exp_f32 v51, v51\n\t" \
  "v_cvt_pk_bf16_f32 v27, v30, v31\n\t" \
  "v_add_f32 v233, v233, v43\n\t" \
  "v_mfma_f32_32x32x16_bf16 v[64:79], a[208:211], a[144:147], v[64:79]\n\t" \
  "ds_read_b64_tr_b16 v[160:161], v228 offset:8192\n\t" \
  "v_add_f32 v234, v234, v28\n\t" \
  "v_exp_f32 v52, v52\n\t" \
  "v_add_f32 v232, v232, v44\n\t" \
  "v_cvt_pk_bf16_f32 v4, v32, v33\n\t" \
  "v_mfma_f32_32x32x16_bf16 v[80:95], a[208:211], a[176:179], v[80:95]\n\t" \
  "ds_read_b64_tr_b16 v[162:163], v228 offset:10240\n\t" \
  "v_add_f32 v235, v235, v29\n\t" \
  "v_exp_f32 v53, v53\n\t" \
  "v_add_f32 v233, v233, v45\n\t" \
  "v_cvt_pk_bf16_f32 v5, v34, v35\n\t" \
  "v_mfma_f32_32x32x16_bf16 v[96:111], a[240:243], a[144:147], v[96:111]\n\t" \
  "ds_read_b64_tr_b16 v[164:165], v228 offset:8704\n\t" \
  "v_add_f32 v234, v234, v30\n\t" \
  "v_exp_f32 v54, v54\n\t" \
  "v_add_f32 v232, v232, v46\n\t" \
  "v_cvt_pk_bf16_f32 v6, v36, v37\n\t" \
  "v_mfma_f32_32x32x16_bf16 v[112:127], a[240:243], a[176:179], v[112:127]\n\t" \
  "ds_read_b64_tr_b16 v[166:167], v228 offset:10752\n\t" \
  "v_add_f32 v235, v235, v31\n\t" \
  "v_exp_f32 v55, v55\n\t" \
  "v_add_f32 v233, v233, v47\n\t" \
  "v_cvt_pk_bf16_f32 v7, v38, v39\n\t" \
  "v_mfma_f32_32x32x16_bf16 v[64:79], a[212:215], a[148:151], v[64:79]\n\t" \
  "ds_read_b64_tr_b16 v[170:171], v228 offset:11264\n\t" \
  "ds_read_b64_tr_b16 v[168:169], v228 offset:9216\n\t" \
  "v_exp_f32 v56, v56\n\t" \
  "v_add_f32 v234, v234, v48\n\t" \
  "v_cvt_pk_bf16_f32 v12, v40, v41\n\t" \
  "v_mfma_f32_32x32x16_bf16 v[80:95], a[212:215], a[180:183], v[80:95]\n\t" \
  "ds_read_b64_tr_b16 v[174:175], v228 offset:11776\n\t" \
  "ds_read_b64_tr_b16 v[172:173], v228 offset:9728\n\t" \
  "v_exp_f32 v57, v57\n\t" \
  "v_add_f32 v235, v235, v49\n\t" \
  "v_cvt_pk_bf16_f32 v13, v42, v43\n\t" \
  "v_mfma_f32_32x32x16_bf16 v[96:111], a[244:247], a[148:151], v[96:111]\n\t" \
  "ds_read_b64_tr_b16 v[178:179], v228 offset:14336\n\t" \
  "ds_read_b64_tr_b16 v[176:177], v228 offset:12288\n\t" \
  "v_exp_f32 v58, v58\n\t" \
  "v_add_f32 v234, v234, v50\n\t" \
  "v_cvt_pk_bf16_f32 v14, v44, v45\n\t" \
  "v_mfma_f32_32x32x16_bf16 v[112:127], a[244:247], a[180:183], v[112:127]\n\t" \
  "ds_read_b64_tr_b16 v[182:183], v228 offset:14848\n\t" \
  "ds_read_b64_tr_b16 v[180:181], v228 offset:12800\n\t" \
  "v_exp_f32 v59, v59\n\t" \
  "v_add_f32 v235, v235, v51\n\t" \
  "v_cvt_pk_bf16_f32 v15, v46, v47\n\t" \
  "v_mfma_f32_32x32x16_bf16 v[64:79], a[216:219], a[152:155], v[64:79]\n\t" \
  "ds_read_b64_tr_b16 v[186:187], v228 offset:15360\n\t" \
  "ds_read_b64_tr_b16 v[184:185], v228 offset:13312\n\t" \
  "v_exp_f32 v60, v60\n\t" \
  "v_add_f32 v234, v234, v52\n\t" \
  "v_cvt_pk_bf16_f32 v20, v48, v49\n\t" \
  "v_mfma_f32_32x32x16_bf16 v[80:95], a[216:219], a[184:187], v[80:95]\n\t" \
  "ds_read_b64_tr_b16 v[190:191], v228 offset:15872\n\t" \
  "ds_read_b64_tr_b16 v[188:189], v228 offset:13824\n\t" \
  "v_exp_f32 v61, v61\n\t" \
  "v_add_f32 v235, v235, v53\n\t" \
  "v_cvt_pk_bf16_f32 v21, v50, v51\n\t" \
  "v_mfma_f32_32x32x16_bf16 v[96:111], a[248:251], a[152:155], v[96:111]\n\t" \
  "v_exp_f32 v62, v62\n\t" \
  "v_add_f32 v234, v234, v54\n\t" \
  "v_cvt_pk_bf16_f32 v22, v52, v53\n\t" \
  "v_mfma_f32_32x32x16_bf16 v[112:127], a[248:251], a[184:187], v[112:127]\n\t" \
  "v_exp_f32 v63, v63\n\t" \
  "v_add_f32 v235, v235, v55\n\t" \
  "v_cvt_pk_bf16_f32 v23, v54, v55\n\t" \
  "v_mfma_f32_32x32x16_bf16 v[64:79], a[220:223], a[156:159], v[64:79]\n\t" \
  "v_add_f32 v234, v234, v56\n\t" \
  "v_add_f32 v235, v235, v57\n\t" \
  "v_cvt_pk_bf16_f32 v28, v56, v57\n\t" \
  "v_mfma_f32_32x32x16_bf16 v[80:95], a[220:223], a[188:191], v[80:95]\n\t" \
  "v_add_f32 v234, v234, v58\n\t" \
  "v_add_f32 v235, v235, v59\n\t" \
  "v_cvt_pk_bf16_f32 v29, v58, v59\n\t" \
  "v_mfma_f32_32x32x16_bf16 v[96:111], a[252:255], a[156:159], v[96:111]\n\t" \
  "v_add_f32 v234, v234, v60\n\t" \
  "v_add_f32 v235, v235, v61\n\t" \
  "v_cvt_pk_bf16_f32 v30, v60, v61\n\t" \
  "v_mfma_f32_32x32x16_bf16 v[112:127], a[252:255], a[188:191], v[112:127]\n\t" \
  "v_add_f32 v234, v234, v62\n\t" \
  "v_add_f32 v235, v235, v63\n\t" \
  "v_cvt_pk_bf16_f32 v31, v62, v63\n\t" \
  "s_waitcnt vmcnt(0) lgkmcnt(0)\n\t" \
  "s_barrier\n\t" \
  "v_mfma_f32_32x32x16_bf16 a[0:15], v[128:131], v[0:3], a[0:15]\n\t" \
  "ds_read_b128 a[196:199], v225 offset:0\n\t" \
  "ds_read_b128 a[192:195], v224 offset:0\n\t" \
  "v_mfma_f32_32x32x16_bf16 a[16:31], v[128:131], v[16:19], a[16:31]\n\t" \
  "ds_read_b128 a[204:207], v227 offset:0\n\t" \
  "ds_read_b128 a[200:203], v226 offset:0\n\t" \
  "v_mfma_f32_32x32x16_bf16 a[32:47], v[132:135], v[0:3], a[32:47]\n\t" \
  "v_max3_f32 v240, v64, v65, v66\n\t" \
  "v_max3_f32 v242, v80, v81, v82\n\t" \
  "v_max3_f32 v241, v96, v97, v98\n\t" \
  "v_max3_f32 v243, v112, v113, v114\n\t" \
  "s_mov_b32 m0, %[m0k1]\n\t" \
  "s_nop 0\n\t" \
  "v_mfma_f32_32x32x16_bf16 a[48:63], v[132:135], v[16:19], a[48:63]\n\t" \
  "v_max3_f32 v240, v240, v67, v68\n\t" \
  "v_max3_f32 v242, v242, v83, v84\n\t" \
  "v_max3_f32 v241, v241, v99, v100\n\t" \
  "v_max3_f32 v243, v243, v115, v116\n\t" \
  "buffer_load_dwordx4 v229, %[srdk], %[sok] offen offset:0 lds\n\t" \
  "buffer_load_dwordx4 v230, %[srdk], %[sok] offen offset:1024 lds\n\t" \
  "v_mfma_f32_32x32x16_bf16 a[64:79], v[136:139], v[0:3], a[64:79]\n\t" \
  "v_max3_f32 v240, v240, v69, v70\n\t" \
  "v_max3_f32 v242, v242, v85, v86\n\t" \
  "v_max3_f32 v241, v241, v101, v102\n\t" \
  "v_max3_f32 v243, v243, v117, v118\n\t" \
  "buffer_load_dwordx4 v229, %[srdk], %[sok] offen offset:2048 lds\n\t" \
  "buffer_load_dwordx4 v230, %[srdk], %[sok] offen offset:3072 lds\n\t" \
  "v_mfma_f32_32x32x16_bf16 a[80:95], v[136:139], v[16:19], a[80:95]\n\t" \
  "v_max3_f32 v240, v240, v71, v72\n\t" \
  "v_max3_f32 v242, v242, v87, v88\n\t" \
  "v_max3_f32 v241, v241, v103, v104\n\t" \
  "v_max3_f32 v243, v243, v119, v120\n\t" \
  "s_mov_b32 m0, %[m0v0]\n\t" \
  "s_nop 0\n\t" \
  "v_mfma_f32_32x32x16_bf16 a[96:111], v[140:143], v[0:3], a[96:111]\n\t" \
  "v_max3_f32 v240, v240, v73, v74\n\t" \
  "v_max3_f32 v242, v242, v89, v90\n\t" \
  "v_max3_f32 v241, v241, v105, v106\n\t" \
  "v_max3_f32 v243, v243, v121, v122\n\t" \
  "buffer_load_dwordx4 v231, %[srdv], %[sov] offen offset:0 lds\n\t" \
  "buffer_load_dwordx4 v231, %[srdv], %[sov] offen offset:2048 lds\n\t" \
  "v_mfma_f32_32x32x16_bf16 a[112:127], v[140:143], v[16:19], a[112:127]\n\t" \
  "v_max3_f32 v240, v240, v75, v76\n\t" \
  "v_max3_f32 v242, v242, v91, v92\n\t" \
  "v_max3_f32 v241, v241, v107, v108\n\t" \
  "v_max3_f32 v243, v243, v123, v124\n\t" \
  "s_add_u32 m0, %[m0v0], 1024\n\t" \
  "s_nop 0\n\t" \
  "v_mfma_f32_32x32x16_bf16 a[0:15], v[144:147], v[8:11], a[0:15]\n\t" \
  "v_max3_f32 v240, v240, v77, v78\n\t" \
  "v_max3_f32 v242, v242, v93, v94\n\t" \
  "v_max3_f32 v241, v241, v109, v110\n\t" \
  "v_max3_f32 v243, v243, v125, v126\n\t" \
  "buffer_load_dwordx4 v231, %[srdv], %[sov2] offen offset:0 lds\n\t" \
  "buffer_load_dwordx4 v231, %[srdv], %[sov2] offen offset:2048 lds\n\t" \
  "v_mfma_f32_32x32x16_bf16 a[16:31], v[144:147], v[24:27], a[16:31]\n\t" \
  "v_max_f32 v240, v240, v79\n\t" \
  "v_max_f32 v242, v242, v95\n\t" \
  "v_max_f32 v241, v241, v111\n\t" \
  "v_max_f32 v243, v243, v127\n\t" \
  "s_add_u32 %[sok], %[sok], 16384\n\t" \
  "s_add_u32 %[sov], %[sov], 16384\n\t" \
  "v_mfma_f32_32x32x16_bf16 a[32:47], v[148:151], v[8:11], a[32:47]\n\t" \
  "s_add_u32 %[sov2], %[sov2], 16384\n\t" \
  "v_max3_f32 v248, v240, v241, v242\n\t" \
  "v_max_f32 v248, v248, v243\n\t" \
  "v_cmp_lt_f32 vcc, %[thr], v248\n\t" \
  "s_mov_b32 %[pend], 0\n\t" \
  "s_nop 0\n\t" \
  "s_cbranch_vccnz .Lx1_%=_RA\n\t" \
  ".Lx1_%=_RAR:\n\t" \
  "v_mfma_f32_32x32x16_bf16 a[48:63], v[148:151], v[24:27], a[48:63]\n\t" \
  "v_exp_f32 v64, v64\n\t" \
  "v_exp_f32 v65, v65\n\t" \
  "v_mfma_f32_32x32x16_bf16 a[64:79], v[152:155], v[8:11], a[64:79]\n\t" \
  "v_exp_f32 v66, v66\n\t" \
  "v_exp_f32 v67, v67\n\t" \
  "v_mfma_f32_32x32x16_bf16 a[80:95], v[152:155], v[24:27], a[80:95]\n\t" \
  "v_exp_f32 v72, v72\n\t" \
  "v_exp_f32 v73, v73\n\t" \
  "v_mfma_f32_32x32x16_bf16 a[96:111], v[156:159], v[8:11], a[96:111]\n\t" \
  "v_add_f32 v232, v232, v64\n\t" \
  "v_exp_f32 v74, v74\n\t" \
  "v_exp_f32 v75, v75\n\t" \
  "v_mfma_f32_32x32x16_bf16 a[112:127], v[156:159], v[24:27], a[112:127]\n\t" \
  "v_add_f32 v233, v233, v65\n\t" \
  "v_exp_f32 v80, v80\n\t" \
  "v_exp_f32 v81, v81\n\t" \
  "v_mfma_f32_32x32x16_bf16 a[0:15], v[160:163], v[4:7], a[0:15]\n\t" \
  "ds_read_b128 a[208:211], v224 offset:128\n\t" \
  "v_add_f32 v232, v232, v66\n\t" \
  "v_exp_f32 v82, v82\n\t" \
  "v_exp_f32 v83, v83\n\t" \
  "v_mfma_f32_32x32x16_bf16 a[16:31], v[160:163], v[20:23], a[16:31]\n\t" \
  "ds_read_b128 a[212:215], v225 offset:128\n\t" \
  "v_add_f32 v233, v233, v67\n\t" \
  "v_exp_f32 v88, v88\n\t" \
  "v_exp_f32 v89, v89\n\t" \
  "v_mfma_f32_32x32x16_bf16 a[32:47], v[164:167], v[4:7], a[32:47]\n\t" \
  "ds_read_b128 a[216:219], v226 offset:128\n\t" \
  "v_add_f32 v232, v232, v72\n\t" \
  "v_exp_f32 v90, v90\n\t" \
  "v_exp_f32 v91, v91\n\t" \
  "v_mfma_f32_32x32x16_bf16 a[48:63], v[164:167], v[20:23], a[48:63]\n\t" \
  "ds_read_b128 a[220:223], v227 offset:128\n\t" \
  "v_add_f32 v233, v233, v73\n\t" \
  "v_exp_f32 v68, v68\n\t" \
  "v_exp_f32 v69, v69\n\t" \
  "v_mfma_f32_32x32x16_bf16 a[64:79], v[168:171], v[4:7], a[64:79]\n\t" \
  "ds_read_b128 a[224:227], v224 offset:8192\n\t" \
  "v_add_f32 v232, v232, v74\n\t" \
  "v_exp_f32 v70, v70\n\t" \
  "v_exp_f32 v71, v71\n\t" \
  "v_mfma_f32_32x32x16_bf16 a[80:95], v[168:171], v[20:23], a[80:95]\n\t" \
  "ds_read_b128 a[228:231], v225 offset:8192\n\t" \
  "v_add_f32 v233, v233, v75\n\t" \
  "v_exp_f32 v76, v76\n\t" \
  "v_exp_f32 v77, v77\n\t" \
  "v_mfma_f32_32x32x16_bf16 a[96:111], v[172:175], v[4:7], a[96:111]\n\t" \
  "ds_read_b128 a[232:235], v226 offset:8192\n\t" \
  "v_add_f32 v234, v234, v80\n\t" \
  "v_exp_f32 v78, v78\n\t" \
  "v_mfma_f32_32x32x16_bf16 a[112:127], v[172:175], v[20:23], a[112:127]\n\t" \
  "ds_read_b128 a[236:239], v227 offset:8192\n\t" \
  "v_add_f32 v235, v235, v81\n\t" \
  "v_exp_f32 v79, v79\n\t" \
  "v_mfma_f32_32x32x16_bf16 a[0:15], v[176:179], v[12:15], a[0:15]\n\t" \
  "ds_read_b128 a[240:243], v224 offset:8320\n\t" \
  "v_add_f32 v234, v234, v82\n\t" \
  "v_exp_f32 v84, v84\n\t" \
  "v_mfma_f32_32x32x16_bf16 a[16:31], v[176:179], v[28:31], a[16:31]\n\t" \
  "ds_read_b128 a[244:247], v225 offset:8320\n\t" \
  "v_add_f32 v235, v235, v83\n\t" \
  "v_exp_f32 v85, v85\n\t" \
  "v_mfma_f32_32x32x16_bf16 a[32:47], v[180:183], v[12:15], a[32:47]\n\t" \
  "ds_read_b128 a[248:251], v226 offset:8320\n\t" \
  "v_add_f32 v234, v234, v88\n\t" \
  "v_exp_f32 v86, v86\n\t" \
  "v_mfma_f32_32x32x16_bf16 a[48:63], v[180:183], v[28:31], a[48:63]\n\t" \
  "ds_read_b128 a[252:255], v227 offset:8320\n\t" \
  "v_add_f32 v235, v235, v89\n\t" \
  "v_exp_f32 v87, v87\n\t" \
  "v_mfma_f32_32x32x16_bf16 a[64:79], v[184:187], v[12:15], a[64:79]\n\t" \
  "v_add_f32 v234, v234, v90\n\t" \
  "v_exp_f32 v92, v92\n\t" \
  "v_mfma_f32_32x32x16_bf16 a[80:95], v[184:187], v[28:31], a[80:95]\n\t" \
  "v_add_f32 v235, v235, v91\n\t" \
  "v_exp_f32 v93, v93\n\t" \
  "v_mfma_f32_32x32x16_bf16 a[96:111], v[188:191], v[12:15], a[96:111]\n\t" \
  "v_exp_f32 v94, v94\n\t" \
  "v_mfma_f32_32x32x16_bf16 a[112:127], v[188:191], v[28:31], a[112:127]\n\t" \
  "v_exp_f32 v95, v95\n\t" \
  "s_cmp_lg_u32 %[pend], 0\n\t" \
  "s_cbranch_scc1 .Lx1_%=_RB\n\t" \
  ".Lx1_%=_RBR:\n\t" \
  "s_waitcnt lgkmcnt(0)\n\t" \
  "s_sub_u32 %[cnt], %[cnt], 1\n\t" \
  "s_cmp_eq_u32 %[cnt], 0\n\t" \
  "s_cbranch_scc1 .LDONE%=\n\t" \
  "v_mfma_f32_32x32x16_bf16 v[0:15], a[192:195], a[128:131], v[192:207]\n\t" \
  "ds_read_b64_tr_b16 v[128:129], v228 offset:16384\n\t" \
  "v_exp_f32 v96, v96\n\t" \
  "v_exp_f32 v97, v97\n\t" \
  "v_cvt_pk_bf16_f32 v64, v64, v65\n\t" \
  "v_mfma_f32_32x32x16_bf16 v[16:31], a[192:195], a[160:163], v[208:223]\n\t" \
  "ds_read_b64_tr_b16 v[130:131], v228 offset:18432\n\t" \
  "v_exp_f32 v98, v98\n\t" \
  "v_exp_f32 v99, v99\n\t" \
  "v_cvt_pk_bf16_f32 v65, v66, v67\n\t" \
  "v_mfma_f32_32x32x16_bf16 v[32:47], a[224:227], a[128:131], v[192:207]\n\t" \
  "ds_read_b64_tr_b16 v[132:133], v228 offset:16896\n\t" \
  "v_exp_f32 v100, v100\n\t" \
  "v_exp_f32 v101, v101\n\t" \
  "v_cvt_pk_bf16_f32 v66, v68, v69\n\t" \
  "v_mfma_f32_32x32x16_bf16 v[48:63], a[224:227], a[160:163], v[208:223]\n\t" \
  "ds_read_b64_tr_b16 v[134:135], v228 offset:18944\n\t" \
  "v_exp_f32 v102, v102\n\t" \
  "v_exp_f32 v103, v103\n\t" \
  "v_cvt_pk_bf16_f32 v67, v70, v71\n\t" \
  "v_mfma_f32_32x32x16_bf16 v[0:15], a[196:199], a[132:135], v[0:15]\n\t" \
  "ds_read_b64_tr_b16 v[136:137], v228 offset:17408\n\t" \
  "v_add_f32 v232, v232, v68\n\t" \
  "v_exp_f32 v104, v104\n\t" \
  "v_cvt_pk_bf16_f32 v72, v72, v73\n\t" \
  "v_add_f32 v232, v232, v96\n\t" \
  "v_mfma_f32_32x32x16_bf16 v[16:31], a[196:199], a[164:167], v[16:31]\n\t" \
  "ds_read_b64_tr_b16 v[138:139], v228 offset:19456\n\t" \
  "v_add_f32 v233, v233, v69\n\t" \
  "v_exp_f32 v105, v105\n\t" \
  "v_cvt_pk_bf16_f32 v73, v74, v75\n\t" \
  "v_add_f32 v233, v233, v97\n\t" \
  "v_mfma_f32_32x32x16_bf16 v[32:47], a[228:231], a[132:135], v[32:47]\n\t" \
  "ds_read_b64_tr_b16 v[140:141], v228 offset:17920\n\t" \
  "v_add_f32 v232, v232, v70\n\t" \
  "v_exp_f32 v106, v106\n\t" \
  "v_cvt_pk_bf16_f32 v74, v76, v77\n\t" \
  "v_add_f32 v232, v232, v98\n\t" \
  "v_mfma_f32_32x32x16_bf16 v[48:63], a[228:231], a[164:167], v[48:63]\n\t" \
  "ds_read_b64_tr_b16 v[142:143], v228 offset:19968\n\t" \
  "v_add_f32 v233, v233, v71\n\t" \
  "v_exp_f32 v107, v107\n\t" \
  "v_cvt_pk_bf16_f32 v75, v78, v79\n\t" \
  "v_add_f32 v233, v233, v99\n\t" \
  "v_mfma_f32_32x32x16_bf16 v[0:15], a[200:203], a[136:139], v[0:15]\n\t" \
  "ds_read_b64_tr_b16 v[144:145], v228 offset:20480\n\t" \
  "v_add_f32 v232, v232, v76\n\t" \
  "v_exp_f32 v108, v108\n\t" \
  "v_cvt_pk_bf16_f32 v80, v80, v81\n\t" \
  "v_add_f32 v232, v232, v100\n\t" \
  "v_mfma_f32_32x32x16_bf16 v[16:31], a[200:203], a[168:171], v[16:31]\n\t" \
  "ds_read_b64_tr_b16 v[146:147], v228 offset:22528\n\t" \
  "v_add_f32 v233, v233, v77\n\t" \
  "v_exp_f32 v109, v109\n\t" \
  "v_cvt_pk_bf16_f32 v81, v82, v83\n\t" \
  "v_add_f32 v233, v233, v101\n\t" \
  "v_mfma_f32_32x32x16_bf16 v[32:47], a[232:235], a[136:139], v[32:47]\n\t" \
  "ds_read_b64_tr_b16 v[148:149], v228 offset:20992\n\t" \
  "v_add_f32 v232, v232, v78\n\t" \
  "v_exp_f32 v110, v110\n\t" \
  "v_cvt_pk_bf16_f32 v82, v84, v85\n\t" \
  "v_add_f32 v232, v232, v102\n\t" \
  "v_mfma_f32_32x32x16_bf16 v[48:63], a[232:235], a[168:171], v[48:63]\n\t" \
  "ds_read_b64_tr_b16 v[150:151], v228 offset:23040\n\t" \
  "v_add_f32 v233, v233, v79\n\t" \
  "v_exp_f32 v111, v111\n\t" \
  "v_cvt_pk_bf16_f32 v83, v86, v87\n\t" \
  "v_add_f32 v233, v233, v103\n\t" \
  "v_mfma_f32_32x32x16_bf16 v[0:15], a[204:207], a[140:143], v[0:15]\n\t" \
  "ds_read_b64_tr_b16 v[152:153], v228 offset:21504\n\t" \
  "v_add_f32 v234, v234, v84\n\t" \
  "v_exp_f32 v112, v112\n\t" \
  "v_cvt_pk_bf16_f32 v88, v88, v89\n\t" \
  "v_add_f32 v232, v232, v104\n\t" \
  "v_mfma_f32_32x32x16_bf16 v[16:31], a[204:207], a[172:175], v[16:31]\n\t" \
  "ds_read_b64_tr_b16 v[154:155], v228 offset:23552\n\t" \
  "v_add_f32 v235, v235, v85\n\t" \
  "v_exp_f32 v113, v113\n\t" \
  "v_cvt_pk_bf16_f32 v89, v90, v91\n\t" \
  "v_add_f32 v233, v233, v105\n\t" \
  "v_mfma_f32_32x32x16_bf16 v[32:47], a[236:239], a[140:143], v[32:47]\n\t" \
  "ds_read_b64_tr_b16 v[156:157], v228 offset:22016\n\t" \
  "v_add_f32 v234, v234, v86\n\t" \
  "v_exp_f32 v114, v114\n\t" \
  "v_cvt_pk_bf16_f32 v90, v92, v93\n\t" \
  "v_add_f32 v232, v232, v106\n\t" \
  "v_mfma_f32_32x32x16_bf16 v[48:63], a[236:239], a[172:175], v[48:63]\n\t" \
  "ds_read_b64_tr_b16 v[158:159], v228 offset:24064\n\t" \
  "v_add_f32 v235, v235, v87\n\t" \
  "v_exp_f32 v115, v115\n\t" \
  "v_cvt_pk_bf16_f32 v91, v94, v95\n\t" \
  "v_add_f32 v233, v233, v107\n\t" \
  "v_mfma_f32_32x32x16_bf16 v[0:15], a[208:211], a[144:147], v[0:15]\n\t" \
  "ds_read_b64_tr_b16 v[160:161], v228 offset:24576\n\t" \
  "v_add_f32 v234, v234, v92\n\t" \
  "v_exp_f32 v116, v116\n\t" \
  "v_add_f32 v232, v232, v108\n\t" \
  "v_cvt_pk_bf16_f32 v68, v96, v97\n\t" \
  "v_mfma_f32_32x32x16_bf16 v[16:31], a[208:211], a[176:179], v[16:31]\n\t" \
  "ds_read_b64_tr_b16 v[162:163], v228 offset:26624\n\t" \
  "v_add_f32 v235, v235, v93\n\t" \
  "v_exp_f32 v117, v117\n\t" \
  "v_add_f32 v233, v233, v109\n\t" \
  "v_cvt_pk_bf16_f32 v69, v98, v99\n\t" \
  "v_mfma_f32_32x32x16_bf16 v[32:47], a[240:243], a[144:147], v[32:47]\n\t" \
  "ds_read_b64_tr_b16 v[164:165], v228 offset:25088\n\t" \
  "v_add_f32 v234, v234, v94\n\t" \
  "v_exp_f32 v118, v118\n\t" \
  "v_add_f32 v232, v232, v110\n\t" \
  "v_cvt_pk_bf16_f32 v70, v100, v101\n\t" \
  "v_mfma_f32_32x32x16_bf16 v[48:63], a[240:243], a[176:179], v[48:63]\n\t" \
  "ds_read_b64_tr_b16 v[166:167], v228 offset:27136\n\t" \
  "v_add_f32 v235, v235, v95\n\t" \
  "v_exp_f32 v119, v119\n\t" \
  "v_add_f32 v233, v233, v111\n\t" \
  "v_cvt_pk_bf16_f32 v71, v102, v103\n\t" \
  "v_mfma_f32_32x32x16_bf16 v[0:15], a[212:215], a[148:151], v[0:15]\n\t" \
  "ds_read_b64_tr_b16 v[170:171], v228 offset:27648\n\t" \
  "ds_read_b64_tr_b16 v[168:169], v228 offset:25600\n\t" \
  "v_exp_f32 v120, v120\n\t" \
  "v_add_f32 v234, v234, v112\n\t" \
  "v_cvt_pk_bf16_f32 v76, v104, v105\n\t" \
  "v_mfma_f32_32x32x16_bf16 v[16:31], a[212:215], a[180:183], v[16:31]\n\t" \
  "ds_read_b64_tr_b16 v[174:175], v228 offset:28160\n\t" \
  "ds_read_b64_tr_b16 v[172:173], v228 offset:26112\n\t" \
  "v_exp_f32 v121, v121\n\t" \
  "v_add_f32 v235, v235, v113\n\t" \
  "v_cvt_pk_bf16_f32 v77, v106, v107\n\t" \
  "v_mfma_f32_32x32x16_bf16 v[32:47], a[244:247], a[148:151], v[32:47]\n\t" \
  "ds_read_b64_tr_b16 v[178:179], v228 offset:30720\n\t" \
  "ds_read_b64_tr_b16 v[176:177], v228 offset:28672\n\t" \
  "v_exp_f32 v122, v122\n\t" \
  "v_add_f32 v234, v234, v114\n\t" \
  "v_cvt_pk_bf16_f32 v78, v108, v109\n\t" \
  "v_mfma_f32_32x32x16_bf16 v[48:63], a[244:247], a[180:183], v[48:63]\n\t" \
  "ds_read_b64_tr_b16 v[182:183], v228 offset:31232\n\t" \
  "ds_read_b64_tr_b16 v[180:181], v228 offset:29184\n\t" \
  "v_exp_f32 v123, v123\n\t" \
  "v_add_f32 v235, v235, v115\n\t" \
  "v_cvt_pk_bf16_f32 v79, v110, v111\n\t" \
  "v_mfma_f32_32x32x16_bf16 v[0:15], a[216:219], a[152:155], v[0:15]\n\t" \
  "ds_read_b64_tr_b16 v[186:187], v228 offset:31744\n\t" \
  "ds_read_b64_tr_b16 v[184:185], v228 offset:29696\n\t" \
  "v_exp_f32 v124, v124\n\t" \
  "v_add_f32 v234, v234, v116\n\t" \
  "v_cvt_pk_bf16_f32 v84, v112, v113\n\t" \
  "v_mfma_f32_32x32x16_bf16 v[16:31], a[216:219], a[184:187], v[16:31]\n\t" \
  "ds_read_b64_tr_b16 v[190:191], v228 offset:32256\n\t" \
  "ds_read_b64_tr_b16 v[188:189], v228 offset:30208\n\t" \
  "v_exp_f32 v125, v125\n\t" \
  "v_add_f32 v235, v235, v117\n\t" \
  "v_cvt_pk_bf16_f32 v85, v114, v115\n\t" \
  "v_mfma_f32_32x32x16_bf16 v[32:47], a[248:251], a[152:155], v[32:47]\n\t" \
  "v_exp_f32 v126, v126\n\t" \
  "v_add_f32 v234, v234, v118\n\t" \
  "v_cvt_pk_bf16_f32 v86, v116, v117\n\t" \
  "v_mfma_f32_32x32x16_bf16 v[48:63], a[248:251], a[184:187], v[48:63]\n\t" \
  "v_exp_f32 v127, v127\n\t" \
  "v_add_f32 v235, v235, v119\n\t" \
  "v_cvt_pk_bf16_f32 v87, v118, v119\n\t" \
  "v_mfma_f32_32x32x16_bf16 v[0:15], a[220:223], a[156:159], v[0:15]\n\t" \
  "v_add_f32 v234, v234, v120\n\t" \
  "v_add_f32 v235, v235, v121\n\t" \
  "v_cvt_pk_bf16_f32 v92, v120, v121\n\t" \
  "v_mfma_f32_32x32x16_bf16 v[16:31], a[220:223], a[188:191], v[16:31]\n\t" \
  "v_add_f32 v234, v234, v122\n\t" \
  "v_add_f32 v235, v235, v123\n\t" \
  "v_cvt_pk_bf16_f32 v93, v122, v123\n\t" \
  "v_mfma_f32_32x32x16_bf16 v[32:47], a[252:255], a[156:159], v[32:47]\n\t" \
  "v_add_f32 v234, v234, v124\n\t" \
  "v_add_f32 v235, v235, v125\n\t" \
  "v_cvt_pk_bf16_f32 v94, v124, v125\n\t" \
  "v_mfma_f32_32x32x16_bf16 v[48:63], a[252:255], a[188:191], v[48:63]\n\t" \
  "v_add_f32 v234, v234, v126\n\t" \
  "v_add_f32 v235, v235, v127\n\t" \
  "v_cvt_pk_bf16_f32 v95, v126, v127\n\t" \
  "s_waitcnt vmcnt(0) lgkmcnt(0)\n\t" \
  "s_barrier\n\t" \
  "v_mfma_f32_32x32x16_bf16 a[0:15], v[128:131], v[64:67], a[0:15]\n\t" \
  "ds_read_b128 a[196:199], v225 offset:16384\n\t" \
  "ds_read_b128 a[192:195], v224 offset:16384\n\t" \
  "v_mfma_f32_32x32x16_bf16 a[16:31], v[128:131], v[80:83], a[16:31]\n\t" \
  "ds_read_b128 a[204:207], v227 offset:16384\n\t" \
  "ds_read_b128 a[200:203], v226 offset:16384\n\t" \
  "v_mfma_f32_32x32x16_bf16 a[32:47], v[132:135], v[64:67], a[32:47]\n\t" \
  "v_max3_f32 v240, v0, v1, v2\n\t" \
  "v_max3_f32 v242, v16, v17, v18\n\t" \
  "v_max3_f32 v241, v32, v33, v34\n\t" \
  "v_max3_f32 v243, v48, v49, v50\n\t" \
  "s_mov_b32 m0, %[m0k0]\n\t" \
  "s_nop 0\n\t" \
  "v_mfma_f32_32x32x16_bf16 a[48:63], v[132:135], v[80:83], a[48:63]\n\t" \
  "v_max3_f32 v240, v240, v3, v4\n\t" \
  "v_max3_f32 v242, v242, v19, v20\n\t" \
  "v_max3_f32 v241, v241, v35, v36\n\t" \
  "v_max3_f32 v243, v243, v51, v52\n\t" \
  "buffer_load_dwordx4 v229, %[srdk], %[sok] offen offset:0 lds\n\t" \
  "buffer_load_dwordx4 v230, %[srdk], %[sok] offen offset:1024 lds\n\t" \
  "v_mfma_f32_32x32x16_bf16 a[64:79], v[136:139], v[64:67], a[64:79]\n\t" \
  "v_max3_f32 v240, v240, v5, v6\n\t" \
  "v_max3_f32 v242, v242, v21, v22\n\t" \
  "v_max3_f32 v241, v241, v37, v38\n\t" \
  "v_max3_f32 v243, v243, v53, v54\n\t" \
  "buffer_load_dwordx4 v229, %[srdk], %[sok] offen offset:2048 lds\n\t" \
  "buffer_load_dwordx4 v230, %[srdk], %[sok] offen offset:3072 lds\n\t" \
  "v_mfma_f32_32x32x16_bf16 a[80:95], v[136:139], v[80:83], a[80:95]\n\t" \
  "v_max3_f32 v240, v240, v7, v8\n\t" \
  "v_max3_f32 v242, v242, v23, v24\n\t" \
  "v_max3_f32 v241, v241, v39, v40\n\t" \
  "v_max3_f32 v243, v243, v55, v56\n\t" \
  "s_mov_b32 m0, %[m0v1]\n\t" \
  "s_nop 0\n\t" \
  "v_mfma_f32_32x32x16_bf16 a[96:111], v[140:143], v[64:67], a[96:111]\n\t" \
  "v_max3_f32 v240, v240, v9, v10\n\t" \
  "v_max3_f32 v242, v242, v25, v26\n\t" \
  "v_max3_f32 v241, v241, v41, v42\n\t" \
  "v_max3_f32 v243, v243, v57, v58\n\t" \
  "buffer_load_dwordx4 v231, %[srdv], %[sov] offen offset:0 lds\n\t" \
  "buffer_load_dwordx4 v231, %[srdv], %[sov] offen offset:2048 lds\n\t" \
  "v_mfma_f32_32x32x16_bf16 a[112:127], v[140:143], v[80:83], a[112:127]\n\t" \
  "v_max3_f32 v240, v240, v11, v12\n\t" \
  "v_max3_f32 v242, v242, v27, v28\n\t" \
  "v_max3_f32 v241, v241, v43, v44\n\t" \
  "v_max3_f32 v243, v243, v59, v60\n\t" \
  "s_add_u32 m0, %[m0v1], 1024\n\t" \
  "s_nop 0\n\t" \
  "v_mfma_f32_32x32x16_bf16 a[0:15], v[144:147], v[72:75], a[0:15]\n\t" \
  "v_max3_f32 v240, v240, v13, v14\n\t" \
  "v_max3_f32 v242, v242, v29, v30\n\t" \
  "v_max3_f32 v241, v241, v45, v46\n\t" \
  "v_max3_f32 v243, v243, v61, v62\n\t" \
  "buffer_load_dwordx4 v231, %[srdv], %[sov2] offen offset:0 lds\n\t" \
  "buffer_load_dwordx4 v231, %[srdv], %[sov2] offen offset:2048 lds\n\t" \
  "v_mfma_f32_32x32x16_bf16 a[16:31], v[144:147], v[88:91], a[16:31]\n\t" \
  "v_max_f32 v240, v240, v15\n\t" \
  "v_max_f32 v242, v242, v31\n\t" \
  "v_max_f32 v241, v241, v47\n\t" \
  "v_max_f32 v243, v243, v63\n\t" \
  "s_add_u32 %[sok], %[sok], 16384\n\t" \
  "s_add_u32 %[sov], %[sov], 16384\n\t" \
  "v_mfma_f32_32x32x16_bf16 a[32:47], v[148:151], v[72:75], a[32:47]\n\t" \
  "s_add_u32 %[sov2], %[sov2], 16384\n\t" \
  "v_max3_f32 v248, v240, v241, v242\n\t" \
  "v_max_f32 v248, v248, v243\n\t" \
  "v_cmp_lt_f32 vcc, %[thr], v248\n\t" \
  "s_mov_b32 %[pend], 0\n\t" \
  "s_nop 0\n\t" \
  "s_cbranch_vccnz .Lx0_%=_RA\n\t" \
  ".Lx0_%=_RAR:\n\t" \
  "v_mfma_f32_32x32x16_bf16 a[48:63], v[148:151], v[88:91], a[48:63]\n\t" \
  "v_exp_f32 v0, v0\n\t" \
  "v_exp_f32 v1, v1\n\t" \
  "v_mfma_f32_32x32x16_bf16 a[64:79], v[152:155], v[72:75], a[64:79]\n\t" \
  "v_exp_f32 v2, v2\n\t" \
  "v_exp_f32 v3, v3\n\t" \
  "v_mfma_f32_32x32x16_bf16 a[80:95], v[152:155], v[88:91], a[80:95]\n\t" \
  "v_exp_f32 v8, v8\n\t" \
  "v_exp_f32 v9, v9\n\t" \
  "v_mfma_f32_32x32x16_bf16 a[96:111], v[156:159], v[72:75], a[96:111]\n\t" \
  "v_add_f32 v232, v232, v0\n\t" \
  "v_exp_f32 v10, v10\n\t" \
  "v_exp_f32 v11, v11\n\t" \
  "v_mfma_f32_32x32x16_bf16 a[112:127], v[156:159], v[88:91], a[112:127]\n\t" \
  "v_add_f32 v233, v233, v1\n\t" \
  "v_exp_f32 v16, v16\n\t" \
  "v_exp_f32 v17, v17\n\t" \
  "v_mfma_f32_32x32x16_bf16 a[0:15], v[160:163], v[68:71], a[0:15]\n\t" \
  "ds_read_b128 a[208:211], v224 offset:16512\n\t" \
  "v_add_f32 v232, v232, v2\n\t" \
  "v_exp_f32 v18, v18\n\t" \
  "v_exp_f32 v19, v19\n\t" \
  "v_mfma_f32_32x32x16_bf16 a[16:31], v[160:163], v[84:87], a[16:31]\n\t" \
  "ds_read_b128 a[212:215], v225 offset:16512\n\t" \
  "v_add_f32 v233, v233, v3\n\t" \
  "v_exp_f32 v24, v24\n\t" \
  "v_exp_f32 v25, v25\n\t" \
  "v_mfma_f32_32x32x16_bf16 a[32:47], v[164:167], v[68:71], a[32:47]\n\t" \
  "ds_read_b128 a[216:219], v226 offset:16512\n\t" \
  "v_add_f32 v232, v232, v8\n\t" \
  "v_exp_f32 v26, v26\n\t" \
  "v_exp_f32 v27, v27\n\t" \
  "v_mfma_f32_32x32x16_bf16 a[48:63], v[164:167], v[84:87], a[48:63]\n\t" \
  "ds_read_b128 a[220:223], v227 offset:16512\n\t" \
  "v_add_f32 v233, v233, v9\n\t" \
  "v_exp_f32 v4, v4\n\t" \
  "v_exp_f32 v5, v5\n\t" \
  "v_mfma_f32_32x32x16_bf16 a[64:79], v[168:171], v[68:71], a[64:79]\n\t" \
  "ds_read_b128 a[224:227], v224 offset:24576\n\t" \
  "v_add_f32 v232, v232, v10\n\t" \
  "v_exp_f32 v6, v6\n\t" \
  "v_exp_f32 v7, v7\n\t" \
  "v_mfma_f32_32x32x16_bf16 a[80:95], v[168:171], v[84:87], a[80:95]\n\t" \
  "ds_read_b128 a[228:231], v225 offset:24576\n\t" \
  "v_add_f32 v233, v233, v11\n\t" \
  "v_exp_f32 v12, v12\n\t" \
  "v_exp_f32 v13, v13\n\t" \
  "v_mfma_f32_32x32x16_bf16 a[96:111], v[172:175], v[68:71], a[96:111]\n\t" \
  "ds_read_b128 a[232:235], v226 offset:24576\n\t" \
  "v_add_f32 v234, v234, v16\n\t" \
  "v_exp_f32 v14, v14\n\t" \
  "v_mfma_f32_32x32x16_bf16 a[112:127], v[172:175], v[84:87], a[112:127]\n\t" \
  "ds_read_b128 a[236:239], v227 offset:24576\n\t" \
  "v_add_f32 v235, v235, v17\n\t" \
  "v_exp_f32 v15, v15\n\t" \
  "v_mfma_f32_32x32x16_bf16 a[0:15], v[176:179], v[76:79], a[0:15]\n\t" \
  "ds_read_b128 a[240:243], v224 offset:24704\n\t" \
  "v_add_f32 v234, v234, v18\n\t" \
  "v_exp_f32 v20, v20\n\t" \
  "v_mfma_f32_32x32x16_bf16 a[16:31], v[176:179], v[92:95], a[16:31]\n\t" \
  "ds_read_b128 a[244:247], v225 offset:24704\n\t" \
  "v_add_f32 v235, v235, v19\n\t" \
  "v_exp_f32 v21, v21\n\t" \
  "v_mfma_f32_32x32x16_bf16 a[32:47], v[180:183], v[76:79], a[32:47]\n\t" \
  "ds_read_b128 a[248:251], v226 offset:24704\n\t" \
  "v_add_f32 v234, v234, v24\n\t" \
  "v_exp_f32 v22, v22\n\t" \
  "v_mfma_f32_32x32x16_bf16 a[48:63], v[180:183], v[92:95], a[48:63]\n\t" \
  "ds_read_b128 a[252:255], v227 offset:24704\n\t" \
  "v_add_f32 v235, v235, v25\n\t" \
  "v_exp_f32 v23, v23\n\t" \
  "v_mfma_f32_32x32x16_bf16 a[64:79], v[184:187], v[76:79], a[64:79]\n\t" \
  "v_add_f32 v234, v234, v26\n\t" \
  "v_exp_f32 v28, v28\n\t" \
  "v_mfma_f32_32x32x16_bf16 a[80:95], v[184:187], v[92:95], a[80:95]\n\t" \
  "v_add_f32 v235, v235, v27\n\t" \
  "v_exp_f32 v29, v29\n\t" \
  "v_mfma_f32_32x32x16_bf16 a[96:111], v[188:191], v[76:79], a[96:111]\n\t" \
  "v_exp_f32 v30, v30\n\t" \
  "v_mfma_f32_32x32x16_bf16 a[112:127], v[188:191], v[92:95], a[112:127]\n\t" \
  "v_exp_f32 v31, v31\n\t" \
  "s_cmp_lg_u32 %[pend], 0\n\t" \
  "s_cbranch_scc1 .Lx0_%=_RB\n\t" \
  ".Lx0_%=_RBR:\n\t" \
  "s_waitcnt lgkmcnt(0)\n\t" \
  "s_branch .LLOOP%=\n\t" \
  ".Lx1_%=_RA:\n\t" \
  "v_max_f32 v248, v240, v241\n\t" \
  "v_max_f32 v249, v242, v243\n\t" \
  "v_mov_b32 v250, v248\n\t" \
  "v_mov_b32 v251, v249\n\t" \
  "s_nop 1\n\t" \
  "v_permlane32_swap_b32 v248, v250\n\t" \
  "v_permlane32_swap_b32 v249, v251\n\t" \
  "v_max_f32 v244, v248, v250\n\t" \
  "v_max_f32 v245, v249, v251\n\t" \
  "v_max_f32 v248, v244, 0\n\t" \
  "v_max_f32 v249, v245, 0\n\t" \
  "v_add_f32 v236, v236, v248\n\t" \
  "v_exp_f32 v246, -v248\n\t" \
  "v_sub_f32 v64, v64, v248\n\t" \
  "v_sub_f32 v65, v65, v248\n\t" \
  "v_sub_f32 v66, v66, v248\n\t" \
  "v_sub_f32 v67, v67, v248\n\t" \
  "v_sub_f32 v68, v68, v248\n\t" \
  "v_sub_f32 v69, v69, v248\n\t" \
  "v_sub_f32 v70, v70, v248\n\t" \
  "v_sub_f32 v71, v71, v248\n\t" \
  "v_sub_f32 v72, v72, v248\n\t" \
  "v_sub_f32 v73, v73, v248\n\t" \
  "v_sub_f32 v74, v74, v248\n\t" \
  "v_sub_f32 v75, v75, v248\n\t" \
  "v_sub_f32 v76, v76, v248\n\t" \
  "v_sub_f32 v77, v77, v248\n\t" \
  "v_sub_f32 v78, v78, v248\n\t" \
  "v_sub_f32 v79, v79, v248\n\t" \
  "v_sub_f32 v96, v96, v248\n\t" \
  "v_sub_f32 v97, v97, v248\n\t" \
  "v_sub_f32 v98, v98, v248\n\t" \
  "v_sub_f32 v99, v99, v248\n\t" \
  "v_sub_f32 v100, v100, v248\n\t" \
  "v_sub_f32 v101, v101, v248\n\t" \
  "v_sub_f32 v102, v102, v248\n\t" \
  "v_sub_f32 v103, v103, v248\n\t" \
  "v_sub_f32 v104, v104, v248\n\t" \
  "v_sub_f32 v105, v105, v248\n\t" \
  "v_sub_f32 v106, v106, v248\n\t" \
  "v_sub_f32 v107, v107, v248\n\t" \
  "v_sub_f32 v108, v108, v248\n\t" \
  "v_sub_f32 v109, v109, v248\n\t" \
  "v_sub_f32 v110, v110, v248\n\t" \
  "v_sub_f32 v111, v111, v248\n\t" \
  "v_sub_f32 v192, 0, v236\n\t" \
  "v_sub_f32 v193, 0, v236\n\t" \
  "v_sub_f32 v194, 0, v236\n\t" \
  "v_sub_f32 v195, 0, v236\n\t" \
  "v_sub_f32 v196, 0, v236\n\t" \
  "v_sub_f32 v197, 0, v236\n\t" \
  "v_sub_f32 v198, 0, v236\n\t" \
  "v_sub_f32 v199, 0, v236\n\t" \
  "v_sub_f32 v200, 0, v236\n\t" \
  "v_sub_f32 v201, 0, v236\n\t" \
  "v_sub_f32 v202, 0, v236\n\t" \
  "v_sub_f32 v203, 0, v236\n\t" \
  "v_sub_f32 v204, 0, v236\n\t" \
  "v_sub_f32 v205, 0, v236\n\t" \
  "v_sub_f32 v206, 0, v236\n\t" \
  "v_sub_f32 v207, 0, v236\n\t" \
  "v_mul_f32 v232, v232, v246\n\t" \
  "v_mul_f32 v233, v233, v246\n\t" \
  "v_add_f32 v237, v237, v249\n\t" \
  "v_exp_f32 v247, -v249\n\t" \
  "v_sub_f32 v80, v80, v249\n\t" \
  "v_sub_f32 v81, v81, v249\n\t" \
  "v_sub_f32 v82, v82, v249\n\t" \
  "v_sub_f32 v83, v83, v249\n\t" \
  "v_sub_f32 v84, v84, v249\n\t" \
  "v_sub_f32 v85, v85, v249\n\t" \
  "v_sub_f32 v86, v86, v249\n\t" \
  "v_sub_f32 v87, v87, v249\n\t" \
  "v_sub_f32 v88, v88, v249\n\t" \
  "v_sub_f32 v89, v89, v249\n\t" \
  "v_sub_f32 v90, v90, v249\n\t" \
  "v_sub_f32 v91, v91, v249\n\t" \
  "v_sub_f32 v92, v92, v249\n\t" \
  "v_sub_f32 v93, v93, v249\n\t" \
  "v_sub_f32 v94, v94, v249\n\t" \
  "v_sub_f32 v95, v95, v249\n\t" \
  "v_sub_f32 v112, v112, v249\n\t" \
  "v_sub_f32 v113, v113, v249\n\t" \
  "v_sub_f32 v114, v114, v249\n\t" \
  "v_sub_f32 v115, v115, v249\n\t" \
  "v_sub_f32 v116, v116, v249\n\t" \
  "v_sub_f32 v117, v117, v249\n\t" \
  "v_sub_f32 v118, v118, v249\n\t" \
  "v_sub_f32 v119, v119, v249\n\t" \
  "v_sub_f32 v120, v120, v249\n\t" \
  "v_sub_f32 v121, v121, v249\n\t" \
  "v_sub_f32 v122, v122, v249\n\t" \
  "v_sub_f32 v123, v123, v249\n\t" \
  "v_sub_f32 v124, v124, v249\n\t" \
  "v_sub_f32 v125, v125, v249\n\t" \
  "v_sub_f32 v126, v126, v249\n\t" \
  "v_sub_f32 v127, v127, v249\n\t" \
  "v_sub_f32 v208, 0, v237\n\t" \
  "v_sub_f32 v209, 0, v237\n\t" \
  "v_sub_f32 v210, 0, v237\n\t" \
  "v_sub_f32 v211, 0, v237\n\t" \
  "v_sub_f32 v212, 0, v237\n\t" \
  "v_sub_f32 v213, 0, v237\n\t" \
  "v_sub_f32 v214, 0, v237\n\t" \
  "v_sub_f32 v215, 0, v237\n\t" \
  "v_sub_f32 v216, 0, v237\n\t" \
  "v_sub_f32 v217, 0, v237\n\t" \
  "v_sub_f32 v218, 0, v237\n\t" \
  "v_sub_f32 v219, 0, v237\n\t" \
  "v_sub_f32 v220, 0, v237\n\t" \
  "v_sub_f32 v221, 0, v237\n\t" \
  "v_sub_f32 v222, 0, v237\n\t" \
  "v_sub_f32 v223, 0, v237\n\t" \
  "v_mul_f32 v234, v234, v247\n\t" \
  "v_mul_f32 v235, v235, v247\n\t" \
  "s_mov_b32 %[pend], 1\n\t" \
  "s_branch .Lx1_%=_RAR\n\t" \
  ".Lx1_%=_RB:\n\t" \
  "s_nop 7\n\t" \
  "s_nop 7\n\t" \
  "v_accvgpr_read_b32 v248, a0\n\t" \
  "v_accvgpr_read_b32 v249, a1\n\t" \
  "v_accvgpr_read_b32 v250, a2\n\t" \
  "v_accvgpr_read_b32 v251, a3\n\t" \
  "v_mul_f32 v248, v246, v248\n\t" \
  "v_mul_f32 v249, v246, v249\n\t" \
  "v_mul_f32 v250, v246, v250\n\t" \
  "v_mul_f32 v251, v246, v251\n\t" \
  "v_accvgpr_write_b32 a0, v248\n\t" \
  "v_accvgpr_write_b32 a1, v249\n\t" \
  "v_accvgpr_write_b32 a2, v250\n\t" \
  "v_accvgpr_write_b32 a3, v251\n\t" \
  "v_accvgpr_read_b32 v248, a4\n\t" \
  "v_accvgpr_read_b32 v249, a5\n\t" \
  "v_accvgpr_read_b32 v250, a6\n\t" \
  "v_accvgpr_read_b32 v251, a7\n\t" \
  "v_mul_f32 v248, v246, v248\n\t" \
  "v_mul_f32 v249, v246, v249\n\t" \
  "v_mul_f32 v250, v246, v250\n\t" \
  "v_mul_f32 v251, v246, v251\n\t" \
  "v_accvgpr_write_b32 a4, v248\n\t" \
  "v_accvgpr_write_b32 a5, v249\n\t" \
  "v_accvgpr_write_b32 a6, v250\n\t" \
  "v_accvgpr_write_b32 a7, v251\n\t" \
  "v_accvgpr_read_b32 v248, a8\n\t" \
  "v_accvgpr_read_b32 v249, a9\n\t" \
  "v_accvgpr_read_b32 v250, a10\n\t" \
  "v_accvgpr_read_b32 v251, a11\n\t" \
  "v_mul_f32 v248, v246, v248\n\t" \
  "v_mul_f32 v249, v246, v249\n\t" \
  "v_mul_f32 v250, v246, v250\n\t" \
  "v_mul_f32 v251, v246, v251\n\t" \
  "v_accvgpr_write_b32 a8, v248\n\t" \
  "v_accvgpr_write_b32 a9, v249\n\t" \
  "v_accvgpr_write_b32 a10, v250\n\t" \
  "v_accvgpr_write_b32 a11, v251\n\t" \
  "v_accvgpr_read_b32 v248, a12\n\t" \
  "v_accvgpr_read_b32 v249, a13\n\t" \
  "v_accvgpr_read_b32 v250, a14\n\t" \
  "v_accvgpr_read_b32 v251, a15\n\t" \
  "v_mul_f32 v248, v246, v248\n\t" \
  "v_mul_f32 v249, v246, v249\n\t" \
  "v_mul_f32 v250, v246, v250\n\t" \
  "v_mul_f32 v251, v246, v251\n\t" \
  "v_accvgpr_write_b32 a12, v248\n\t" \
  "v_accvgpr_write_b32 a13, v249\n\t" \
  "v_accvgpr_write_b32 a14, v250\n\t" \
  "v_accvgpr_write_b32 a15, v251\n\t" \
  "v_accvgpr_read_b32 v248, a16\n\t" \
  "v_accvgpr_read_b32 v249, a17\n\t" \
  "v_accvgpr_read_b32 v250, a18\n\t" \
  "v_accvgpr_read_b32 v251, a19\n\t" \
  "v_mul_f32 v248, v247, v248\n\t" \
  "v_mul_f32 v249, v247, v249\n\t" \
  "v_mul_f32 v250, v247, v250\n\t" \
  "v_mul_f32 v251, v247, v251\n\t" \
  "v_accvgpr_write_b32 a16, v248\n\t" \
  "v_accvgpr_write_b32 a17, v249\n\t" \
  "v_accvgpr_write_b32 a18, v250\n\t" \
  "v_accvgpr_write_b32 a19, v251\n\t" \
  "v_accvgpr_read_b32 v248, a20\n\t" \
  "v_accvgpr_read_b32 v249, a21\n\t" \
  "v_accvgpr_read_b32 v250, a22\n\t" \
  "v_accvgpr_read_b32 v251, a23\n\t" \
  "v_mul_f32 v248, v247, v248\n\t" \
  "v_mul_f32 v249, v247, v249\n\t" \
  "v_mul_f32 v250, v247, v250\n\t" \
  "v_mul_f32 v251, v247, v251\n\t" \
  "v_accvgpr_write_b32 a20, v248\n\t" \
  "v_accvgpr_write_b32 a21, v249\n\t" \
  "v_accvgpr_write_b32 a22, v250\n\t" \
  "v_accvgpr_write_b32 a23, v251\n\t" \
  "v_accvgpr_read_b32 v248, a24\n\t" \
  "v_accvgpr_read_b32 v249, a25\n\t" \
  "v_accvgpr_read_b32 v250, a26\n\t" \
  "v_accvgpr_read_b32 v251, a27\n\t" \
  "v_mul_f32 v248, v247, v248\n\t" \
  "v_mul_f32 v249, v247, v249\n\t" \
  "v_mul_f32 v250, v247, v250\n\t" \
  "v_mul_f32 v251, v247, v251\n\t" \
  "v_accvgpr_write_b32 a24, v248\n\t" \
  "v_accvgpr_write_b32 a25, v249\n\t" \
  "v_accvgpr_write_b32 a26, v250\n\t" \
  "v_accvgpr_write_b32 a27, v251\n\t" \
  "v_accvgpr_read_b32 v248, a28\n\t" \
  "v_accvgpr_read_b32 v249, a29\n\t" \
  "v_accvgpr_read_b32 v250, a30\n\t" \
  "v_accvgpr_read_b32 v251, a31\n\t" \
  "v_mul_f32 v248, v247, v248\n\t" \
  "v_mul_f32 v249, v247, v249\n\t" \
  "v_mul_f32 v250, v247, v250\n\t" \
  "v_mul_f32 v251, v247, v251\n\t" \
  "v_accvgpr_write_b32 a28, v248\n\t" \
  "v_accvgpr_write_b32 a29, v249\n\t" \
  "v_accvgpr_write_b32 a30, v250\n\t" \
  "v_accvgpr_write_b32 a31, v251\n\t" \
  "v_accvgpr_read_b32 v248, a32\n\t" \
  "v_accvgpr_read_b32 v249, a33\n\t" \
  "v_accvgpr_read_b32 v250, a34\n\t" \
  "v_accvgpr_read_b32 v251, a35\n\t" \
  "v_mul_f32 v248, v246, v248\n\t" \
  "v_mul_f32 v249, v246, v249\n\t" \
  "v_mul_f32 v250, v246, v250\n\t" \
  "v_mul_f32 v251, v246, v251\n\t" \
  "v_accvgpr_write_b32 a32, v248\n\t" \
  "v_accvgpr_write_b32 a33, v249\n\t" \
  "v_accvgpr_write_b32 a34, v250\n\t" \
  "v_accvgpr_write_b32 a35, v251\n\t" \
  "v_accvgpr_read_b32 v248, a36\n\t" \
  "v_accvgpr_read_b32 v249, a37\n\t" \
  "v_accvgpr_read_b32 v250, a38\n\t" \
  "v_accvgpr_read_b32 v251, a39\n\t" \
  "v_mul_f32 v248, v246, v248\n\t" \
  "v_mul_f32 v249, v246, v249\n\t" \
  "v_mul_f32 v250, v246, v250\n\t" \
  "v_mul_f32 v251, v246, v251\n\t" \
  "v_accvgpr_write_b32 a36, v248\n\t" \
  "v_accvgpr_write_b32 a37, v249\n\t" \
  "v_accvgpr_write_b32 a38, v250\n\t" \
  "v_accvgpr_write_b32 a39, v251\n\t" \
  "v_accvgpr_read_b32 v248, a40\n\t" \
  "v_accvgpr_read_b32 v249, a41\n\t" \
  "v_accvgpr_read_b32 v250, a42\n\t" \
  "v_accvgpr_read_b32 v251, a43\n\t" \
  "v_mul_f32 v248, v246, v248\n\t" \
  "v_mul_f32 v249, v246, v249\n\t" \
  "v_mul_f32 v250, v246, v250\n\t" \
  "v_mul_f32 v251, v246, v251\n\t" \
  "v_accvgpr_write_b32 a40, v248\n\t" \
  "v_accvgpr_write_b32 a41, v249\n\t" \
  "v_accvgpr_write_b32 a42, v250\n\t" \
  "v_accvgpr_write_b32 a43, v251\n\t" \
  "v_accvgpr_read_b32 v248, a44\n\t" \
  "v_accvgpr_read_b32 v249, a45\n\t" \
  "v_accvgpr_read_b32 v250, a46\n\t" \
  "v_accvgpr_read_b32 v251, a47\n\t" \
  "v_mul_f32 v248, v246, v248\n\t" \
  "v_mul_f32 v249, v246, v249\n\t" \
  "v_mul_f32 v250, v246, v250\n\t" \
  "v_mul_f32 v251, v246, v251\n\t" \
  "v_accvgpr_write_b32 a44, v248\n\t" \
  "v_accvgpr_write_b32 a45, v249\n\t" \
  "v_accvgpr_write_b32 a46, v250\n\t" \
  "v_accvgpr_write_b32 a47, v251\n\t" \
  "v_accvgpr_read_b32 v248, a48\n\t" \
  "v_accvgpr_read_b32 v249, a49\n\t" \
  "v_accvgpr_read_b32 v250, a50\n\t" \
  "v_accvgpr_read_b32 v251, a51\n\t" \
  "v_mul_f32 v248, v247, v248\n\t" \
  "v_mul_f32 v249, v247, v249\n\t" \
  "v_mul_f32 v250, v247, v250\n\t" \
  "v_mul_f32 v251, v247, v251\n\t" \
  "v_accvgpr_write_b32 a48, v248\n\t" \
  "v_accvgpr_write_b32 a49, v249\n\t" \
  "v_accvgpr_write_b32 a50, v250\n\t" \
  "v_accvgpr_write_b32 a51, v251\n\t" \
  "v_accvgpr_read_b32 v248, a52\n\t" \
  "v_accvgpr_read_b32 v249, a53\n\t" \
  "v_accvgpr_read_b32 v250, a54\n\t" \
  "v_accvgpr_read_b32 v251, a55\n\t" \
  "v_mul_f32 v248, v247, v248\n\t" \
  "v_mul_f32 v249, v247, v249\n\t" \
  "v_mul_f32 v250, v247, v250\n\t" \
  "v_mul_f32 v251, v247, v251\n\t" \
  "v_accvgpr_write_b32 a52, v248\n\t" \
  "v_accvgpr_write_b32 a53, v249\n\t" \
  "v_accvgpr_write_b32 a54, v250\n\t" \
  "v_accvgpr_write_b32 a55, v251\n\t" \
  "v_accvgpr_read_b32 v248, a56\n\t" \
  "v_accvgpr_read_b32 v249, a57\n\t" \
  "v_accvgpr_read_b32 v250, a58\n\t" \
  "v_accvgpr_read_b32 v251, a59\n\t" \
  "v_mul_f32 v248, v247, v248\n\t" \
  "v_mul_f32 v249, v247, v249\n\t" \
  "v_mul_f32 v250, v247, v250\n\t" \
  "v_mul_f32 v251, v247, v251\n\t" \
  "v_accvgpr_write_b32 a56, v248\n\t" \
  "v_accvgpr_write_b32 a57, v249\n\t" \
  "v_accvgpr_write_b32 a58, v250\n\t" \
  "v_accvgpr_write_b32 a59, v251\n\t" \
  "v_accvgpr_read_b32 v248, a60\n\t" \
  "v_accvgpr_read_b32 v249, a61\n\t" \
  "v_accvgpr_read_b32 v250, a62\n\t" \
  "v_accvgpr_read_b32 v251, a63\n\t" \
  "v_mul_f32 v248, v247, v248\n\t" \
  "v_mul_f32 v249, v247, v249\n\t" \
  "v_mul_f32 v250, v247, v250\n\t" \
  "v_mul_f32 v251, v247, v251\n\t" \
  "v_accvgpr_write_b32 a60, v248\n\t" \
  "v_accvgpr_write_b32 a61, v249\n\t" \
  "v_accvgpr_write_b32 a62, v250\n\t" \
  "v_accvgpr_write_b32 a63, v251\n\t" \
  "v_accvgpr_read_b32 v248, a64\n\t" \
  "v_accvgpr_read_b32 v249, a65\n\t" \
  "v_accvgpr_read_b32 v250, a66\n\t" \
  "v_accvgpr_read_b32 v251, a67\n\t" \
  "v_mul_f32 v248, v246, v248\n\t" \
  "v_mul_f32 v249, v246, v249\n\t" \
  "v_mul_f32 v250, v246, v250\n\t" \
  "v_mul_f32 v251, v246, v251\n\t" \
  "v_accvgpr_write_b32 a64, v248\n\t" \
  "v_accvgpr_write_b32 a65, v249\n\t" \
  "v_accvgpr_write_b32 a66, v250\n\t" \
  "v_accvgpr_write_b32 a67, v251\n\t" \
  "v_accvgpr_read_b32 v248, a68\n\t" \
  "v_accvgpr_read_b32 v249, a69\n\t" \
  "v_accvgpr_read_b32 v250, a70\n\t" \
  "v_accvgpr_read_b32 v251, a71\n\t" \
  "v_mul_f32 v248, v246, v248\n\t" \
  "v_mul_f32 v249, v246, v249\n\t" \
  "v_mul_f32 v250, v246, v250\n\t" \
  "v_mul_f32 v251, v246, v251\n\t" \
  "v_accvgpr_write_b32 a68, v248\n\t" \
  "v_accvgpr_write_b32 a69, v249\n\t" \
  "v_accvgpr_write_b32 a70, v250\n\t" \
  "v_accvgpr_write_b32 a71, v251\n\t" \
  "v_accvgpr_read_b32 v248, a72\n\t" \
  "v_accvgpr_read_b32 v249, a73\n\t" \
  "v_accvgpr_read_b32 v250, a74\n\t" \
  "v_accvgpr_read_b32 v251, a75\n\t" \
  "v_mul_f32 v248, v246, v248\n\t" \
  "v_mul_f32 v249, v246, v249\n\t" \
  "v_mul_f32 v250, v246, v250\n\t" \
  "v_mul_f32 v251, v246, v251\n\t" \
  "v_accvgpr_write_b32 a72, v248\n\t" \
  "v_accvgpr_write_b32 a73, v249\n\t" \
  "v_accvgpr_write_b32 a74, v250\n\t" \
  "v_accvgpr_write_b32 a75, v251\n\t" \
  "v_accvgpr_read_b32 v248, a76\n\t" \
  "v_accvgpr_read_b32 v249, a77\n\t" \
  "v_accvgpr_read_b32 v250, a78\n\t" \
  "v_accvgpr_read_b32 v251, a79\n\t" \
  "v_mul_f32 v248, v246, v248\n\t" \
  "v_mul_f32 v249, v246, v249\n\t" \
  "v_mul_f32 v250, v246, v250\n\t" \
  "v_mul_f32 v251, v246, v251\n\t" \
  "v_accvgpr_write_b32 a76, v248\n\t" \
  "v_accvgpr_write_b32 a77, v249\n\t" \
  "v_accvgpr_write_b32 a78, v250\n\t" \
  "v_accvgpr_write_b32 a79, v251\n\t" \
  "v_accvgpr_read_b32 v248, a80\n\t" \
  "v_accvgpr_read_b32 v249, a81\n\t" \
  "v_accvgpr_read_b32 v250, a82\n\t" \
  "v_accvgpr_read_b32 v251, a83\n\t" \
  "v_mul_f32 v248, v247, v248\n\t" \
  "v_mul_f32 v249, v247, v249\n\t" \
  "v_mul_f32 v250, v247, v250\n\t" \
  "v_mul_f32 v251, v247, v251\n\t" \
  "v_accvgpr_write_b32 a80, v248\n\t" \
  "v_accvgpr_write_b32 a81, v249\n\t" \
  "v_accvgpr_write_b32 a82, v250\n\t" \
  "v_accvgpr_write_b32 a83, v251\n\t" \
  "v_accvgpr_read_b32 v248, a84\n\t" \
  "v_accvgpr_read_b32 v249, a85\n\t" \
  "v_accvgpr_read_b32 v250, a86\n\t" \
  "v_accvgpr_read_b32 v251, a87\n\t" \
  "v_mul_f32 v248, v247, v248\n\t" \
  "v_mul_f32 v249, v247, v249\n\t" \
  "v_mul_f32 v250, v247, v250\n\t" \
  "v_mul_f32 v251, v247, v251\n\t" \
  "v_accvgpr_write_b32 a84, v248\n\t" \
  "v_accvgpr_write_b32 a85, v249\n\t" \
  "v_accvgpr_write_b32 a86, v250\n\t" \
  "v_accvgpr_write_b32 a87, v251\n\t" \
  "v_accvgpr_read_b32 v248, a88\n\t" \
  "v_accvgpr_read_b32 v249, a89\n\t" \
  "v_accvgpr_read_b32 v250, a90\n\t" \
  "v_accvgpr_read_b32 v251, a91\n\t" \
  "v_mul_f32 v248, v247, v248\n\t" \
  "v_mul_f32 v249, v247, v249\n\t" \
  "v_mul_f32 v250, v247, v250\n\t" \
  "v_mul_f32 v251, v247, v251\n\t" \
  "v_accvgpr_write_b32 a88, v248\n\t" \
  "v_accvgpr_write_b32 a89, v249\n\t" \
  "v_accvgpr_write_b32 a90, v250\n\t" \
  "v_accvgpr_write_b32 a91, v251\n\t" \
  "v_accvgpr_read_b32 v248, a92\n\t" \
  "v_accvgpr_read_b32 v249, a93\n\t" \
  "v_accvgpr_read_b32 v250, a94\n\t" \
  "v_accvgpr_read_b32 v251, a95\n\t" \
  "v_mul_f32 v248, v247, v248\n\t" \
  "v_mul_f32 v249, v247, v249\n\t" \
  "v_mul_f32 v250, v247, v250\n\t" \
  "v_mul_f32 v251, v247, v251\n\t" \
  "v_accvgpr_write_b32 a92, v248\n\t" \
  "v_accvgpr_write_b32 a93, v249\n\t" \
  "v_accvgpr_write_b32 a94, v250\n\t" \
  "v_accvgpr_write_b32 a95, v251\n\t" \
  "v_accvgpr_read_b32 v248, a96\n\t" \
  "v_accvgpr_read_b32 v249, a97\n\t" \
  "v_accvgpr_read_b32 v250, a98\n\t" \
  "v_accvgpr_read_b32 v251, a99\n\t" \
  "v_mul_f32 v248, v246, v248\n\t" \
  "v_mul_f32 v249, v246, v249\n\t" \
  "v_mul_f32 v250, v246, v250\n\t" \
  "v_mul_f32 v251, v246, v251\n\t" \
  "v_accvgpr_write_b32 a96, v248\n\t" \
  "v_accvgpr_write_b32 a97, v249\n\t" \
  "v_accvgpr_write_b32 a98, v250\n\t" \
  "v_accvgpr_write_b32 a99, v251\n\t" \
  "v_accvgpr_read_b32 v248, a100\n\t" \
  "v_accvgpr_read_b32 v249, a101\n\t" \
  "v_accvgpr_read_b32 v250, a102\n\t" \
  "v_accvgpr_read_b32 v251, a103\n\t" \
  "v_mul_f32 v248, v246, v248\n\t" \
  "v_mul_f32 v249, v246, v249\n\t" \
  "v_mul_f32 v250, v246, v250\n\t" \
  "v_mul_f32 v251, v246, v251\n\t" \
  "v_accvgpr_write_b32 a100, v248\n\t" \
  "v_accvgpr_write_b32 a101, v249\n\t" \
  "v_accvgpr_write_b32 a102, v250\n\t" \
  "v_accvgpr_write_b32 a103, v251\n\t" \
  "v_accvgpr_read_b32 v248, a104\n\t" \
  "v_accvgpr_read_b32 v249, a105\n\t" \
  "v_accvgpr_read_b32 v250, a106\n\t" \
  "v_accvgpr_read_b32 v251, a107\n\t" \
  "v_mul_f32 v248, v246, v248\n\t" \
  "v_mul_f32 v249, v246, v249\n\t" \
  "v_mul_f32 v250, v246, v250\n\t" \
  "v_mul_f32 v251, v246, v251\n\t" \
  "v_accvgpr_write_b32 a104, v248\n\t" \
  "v_accvgpr_write_b32 a105, v249\n\t" \
  "v_accvgpr_write_b32 a106, v250\n\t" \
  "v_accvgpr_write_b32 a107, v251\n\t" \
  "v_accvgpr_read_b32 v248, a108\n\t" \
  "v_accvgpr_read_b32 v249, a109\n\t" \
  "v_accvgpr_read_b32 v250, a110\n\t" \
  "v_accvgpr_read_b32 v251, a111\n\t" \
  "v_mul_f32 v248, v246, v248\n\t" \
  "v_mul_f32 v249, v246, v249\n\t" \
  "v_mul_f32 v250, v246, v250\n\t" \
  "v_mul_f32 v251, v246, v251\n\t" \
  "v_accvgpr_write_b32 a108, v248\n\t" \
  "v_accvgpr_write_b32 a109, v249\n\t" \
  "v_accvgpr_write_b32 a110, v250\n\t" \
  "v_accvgpr_write_b32 a111, v251\n\t" \
  "v_accvgpr_read_b32 v248, a112\n\t" \
  "v_accvgpr_read_b32 v249, a113\n\t" \
  "v_accvgpr_read_b32 v250, a114\n\t" \
  "v_accvgpr_read_b32 v251, a115\n\t" \
  "v_mul_f32 v248, v247, v248\n\t" \
  "v_mul_f32 v249, v247, v249\n\t" \
  "v_mul_f32 v250, v247, v250\n\t" \
  "v_mul_f32 v251, v247, v251\n\t" \
  "v_accvgpr_write_b32 a112, v248\n\t" \
  "v_accvgpr_write_b32 a113, v249\n\t" \
  "v_accvgpr_write_b32 a114, v250\n\t" \
  "v_accvgpr_write_b32 a115, v251\n\t" \
  "v_accvgpr_read_b32 v248, a116\n\t" \
  "v_accvgpr_read_b32 v249, a117\n\t" \
  "v_accvgpr_read_b32 v250, a118\n\t" \
  "v_accvgpr_read_b32 v251, a119\n\t" \
  "v_mul_f32 v248, v247, v248\n\t" \
  "v_mul_f32 v249, v247, v249\n\t" \
  "v_mul_f32 v250, v247, v250\n\t" \
  "v_mul_f32 v251, v247, v251\n\t" \
  "v_accvgpr_write_b32 a116, v248\n\t" \
  "v_accvgpr_write_b32 a117, v249\n\t" \
  "v_accvgpr_write_b32 a118, v250\n\t" \
  "v_accvgpr_write_b32 a119, v251\n\t" \
  "v_accvgpr_read_b32 v248, a120\n\t" \
  "v_accvgpr_read_b32 v249, a121\n\t" \
  "v_accvgpr_read_b32 v250, a122\n\t" \
  "v_accvgpr_read_b32 v251, a123\n\t" \
  "v_mul_f32 v248, v247, v248\n\t" \
  "v_mul_f32 v249, v247, v249\n\t" \
  "v_mul_f32 v250, v247, v250\n\t" \
  "v_mul_f32 v251, v247, v251\n\t" \
  "v_accvgpr_write_b32 a120, v248\n\t" \
  "v_accvgpr_write_b32 a121, v249\n\t" \
  "v_accvgpr_write_b32 a122, v250\n\t" \
  "v_accvgpr_write_b32 a123, v251\n\t" \
  "v_accvgpr_read_b32 v248, a124\n\t" \
  "v_accvgpr_read_b32 v249, a125\n\t" \
  "v_accvgpr_read_b32 v250, a126\n\t" \
  "v_accvgpr_read_b32 v251, a127\n\t" \
  "v_mul_f32 v248, v247, v248\n\t" \
  "v_mul_f32 v249, v247, v249\n\t" \
  "v_mul_f32 v250, v247, v250\n\t" \
  "v_mul_f32 v251, v247, v251\n\t" \
  "v_accvgpr_write_b32 a124, v248\n\t" \
  "v_accvgpr_write_b32 a125, v249\n\t" \
  "v_accvgpr_write_b32 a126, v250\n\t" \
  "v_accvgpr_write_b32 a127, v251\n\t" \
  "s_nop 1\n\t" \
  "s_branch .Lx1_%=_RBR\n\t" \
  ".Lx0_%=_RA:\n\t" \
  "v_max_f32 v248, v240, v241\n\t" \
  "v_max_f32 v249, v242, v243\n\t" \
  "v_mov_b32 v250, v248\n\t" \
  "v_mov_b32 v251, v249\n\t" \
  "s_nop 1\n\t" \
  "v_permlane32_swap_b32 v248, v250\n\t" \
  "v_permlane32_swap_b32 v249, v251\n\t" \
  "v_max_f32 v244, v248, v250\n\t" \
  "v_max_f32 v245, v249, v251\n\t" \
  "v_max_f32 v248, v244, 0\n\t" \
  "v_max_f32 v249, v245, 0\n\t" \
  "v_add_f32 v236, v236, v248\n\t" \
  "v_exp_f32 v246, -v248\n\t" \
  "v_sub_f32 v0, v0, v248\n\t" \
  "v_sub_f32 v1, v1, v248\n\t" \
  "v_sub_f32 v2, v2, v248\n\t" \
  "v_sub_f32 v3, v3, v248\n\t" \
  "v_sub_f32 v4, v4, v248\n\t" \
  "v_sub_f32 v5, v5, v248\n\t" \
  "v_sub_f32 v6, v6, v248\n\t" \
  "v_sub_f32 v7, v7, v248\n\t" \
  "v_sub_f32 v8, v8, v248\n\t" \
  "v_sub_f32 v9, v9, v248\n\t" \
  "v_sub_f32 v10, v10, v248\n\t" \
  "v_sub_f32 v11, v11, v248\n\t" \
  "v_sub_f32 v12, v12, v248\n\t" \
  "v_sub_f32 v13, v13, v248\n\t" \
  "v_sub_f32 v14, v14, v248\n\t" \
  "v_sub_f32 v15, v15, v248\n\t" \
  "v_sub_f32 v32, v32, v248\n\t" \
  "v_sub_f32 v33, v33, v248\n\t" \
  "v_sub_f32 v34, v34, v248\n\t" \
  "v_sub_f32 v35, v35, v248\n\t" \
  "v_sub_f32 v36, v36, v248\n\t" \
  "v_sub_f32 v37, v37, v248\n\t" \
  "v_sub_f32 v38, v38, v248\n\t" \
  "v_sub_f32 v39, v39, v248\n\t" \
  "v_sub_f32 v40, v40, v248\n\t" \
  "v_sub_f32 v41, v41, v248\n\t" \
  "v_sub_f32 v42, v42, v248\n\t" \
  "v_sub_f32 v43, v43, v248\n\t" \
  "v_sub_f32 v44, v44, v248\n\t" \
  "v_sub_f32 v45, v45, v248\n\t" \
  "v_sub_f32 v46, v46, v248\n\t" \
  "v_sub_f32 v47, v47, v248\n\t" \
  "v_sub_f32 v192, 0, v236\n\t" \
  "v_sub_f32 v193, 0, v236\n\t" \
  "v_sub_f32 v194, 0, v236\n\t" \
  "v_sub_f32 v195, 0, v236\n\t" \
  "v_sub_f32 v196, 0, v236\n\t" \
  "v_sub_f32 v197, 0, v236\n\t" \
  "v_sub_f32 v198, 0, v236\n\t" \
  "v_sub_f32 v199, 0, v236\n\t" \
  "v_sub_f32 v200, 0, v236\n\t" \
  "v_sub_f32 v201, 0, v236\n\t" \
  "v_sub_f32 v202, 0, v236\n\t" \
  "v_sub_f32 v203, 0, v236\n\t" \
  "v_sub_f32 v204, 0, v236\n\t" \
  "v_sub_f32 v205, 0, v236\n\t" \
  "v_sub_f32 v206, 0, v236\n\t" \
  "v_sub_f32 v207, 0, v236\n\t" \
  "v_mul_f32 v232, v232, v246\n\t" \
  "v_mul_f32 v233, v233, v246\n\t" \
  "v_add_f32 v237, v237, v249\n\t" \
  "v_exp_f32 v247, -v249\n\t" \
  "v_sub_f32 v16, v16, v249\n\t" \
  "v_sub_f32 v17, v17, v249\n\t" \
  "v_sub_f32 v18, v18, v249\n\t" \
  "v_sub_f32 v19, v19, v249\n\t" \
  "v_sub_f32 v20, v20, v249\n\t" \
  "v_sub_f32 v21, v21, v249\n\t" \
  "v_sub_f32 v22, v22, v249\n\t" \
  "v_sub_f32 v23, v23, v249\n\t" \
  "v_sub_f32 v24, v24, v249\n\t" \
  "v_sub_f32 v25, v25, v249\n\t" \
  "v_sub_f32 v26, v26, v249\n\t" \
  "v_sub_f32 v27, v27, v249\n\t" \
  "v_sub_f32 v28, v28, v249\n\t" \
  "v_sub_f32 v29, v29, v249\n\t" \
  "v_sub_f32 v30, v30, v249\n\t" \
  "v_sub_f32 v31, v31, v249\n\t" \
  "v_sub_f32 v48, v48, v249\n\t" \
  "v_sub_f32 v49, v49, v249\n\t" \
  "v_sub_f32 v50, v50, v249\n\t" \
  "v_sub_f32 v51, v51, v249\n\t" \
  "v_sub_f32 v52, v52, v249\n\t" \
  "v_sub_f32 v53, v53, v249\n\t" \
  "v_sub_f32 v54, v54, v249\n\t" \
  "v_sub_f32 v55, v55, v249\n\t" \
  "v_sub_f32 v56, v56, v249\n\t" \
  "v_sub_f32 v57, v57, v249\n\t" \
  "v_sub_f32 v58, v58, v249\n\t" \
  "v_sub_f32 v59, v59, v249\n\t" \
  "v_sub_f32 v60, v60, v249\n\t" \
  "v_sub_f32 v61, v61, v249\n\t" \
  "v_sub_f32 v62, v62, v249\n\t" \
  "v_sub_f32 v63, v63, v249\n\t" \
  "v_sub_f32 v208, 0, v237\n\t" \
  "v_sub_f32 v209, 0, v237\n\t" \
  "v_sub_f32 v210, 0, v237\n\t" \
  "v_sub_f32 v211, 0, v237\n\t" \
  "v_sub_f32 v212, 0, v237\n\t" \
  "v_sub_f32 v213, 0, v237\n\t" \
  "v_sub_f32 v214, 0, v237\n\t" \
  "v_sub_f32 v215, 0, v237\n\t" \
  "v_sub_f32 v216, 0, v237\n\t" \
  "v_sub_f32 v217, 0, v237\n\t" \
  "v_sub_f32 v218, 0, v237\n\t" \
  "v_sub_f32 v219, 0, v237\n\t" \
  "v_sub_f32 v220, 0, v237\n\t" \
  "v_sub_f32 v221, 0, v237\n\t" \
  "v_sub_f32 v222, 0, v237\n\t" \
  "v_sub_f32 v223, 0, v237\n\t" \
  "v_mul_f32 v234, v234, v247\n\t" \
  "v_mul_f32 v235, v235, v247\n\t" \
  "s_mov_b32 %[pend], 1\n\t" \
  "s_branch .Lx0_%=_RAR\n\t" \
  ".Lx0_%=_RB:\n\t" \
  "s_nop 7\n\t" \
  "s_nop 7\n\t" \
  "v_accvgpr_read_b32 v248, a0\n\t" \
  "v_accvgpr_read_b32 v249, a1\n\t" \
  "v_accvgpr_read_b32 v250, a2\n\t" \
  "v_accvgpr_read_b32 v251, a3\n\t" \
  "v_mul_f32 v248, v246, v248\n\t" \
  "v_mul_f32 v249, v246, v249\n\t" \
  "v_mul_f32 v250, v246, v250\n\t" \
  "v_mul_f32 v251, v246, v251\n\t" \
  "v_accvgpr_write_b32 a0, v248\n\t" \
  "v_accvgpr_write_b32 a1, v249\n\t" \
  "v_accvgpr_write_b32 a2, v250\n\t" \
  "v_accvgpr_write_b32 a3, v251\n\t" \
  "v_accvgpr_read_b32 v248, a4\n\t" \
  "v_accvgpr_read_b32 v249, a5\n\t" \
  "v_accvgpr_read_b32 v250, a6\n\t" \
  "v_accvgpr_read_b32 v251, a7\n\t" \
  "v_mul_f32 v248, v246, v248\n\t" \
  "v_mul_f32 v249, v246, v249\n\t" \
  "v_mul_f32 v250, v246, v250\n\t" \
  "v_mul_f32 v251, v246, v251\n\t" \
  "v_accvgpr_write_b32 a4, v248\n\t" \
  "v_accvgpr_write_b32 a5, v249\n\t" \
  "v_accvgpr_write_b32 a6, v250\n\t" \
  "v_accvgpr_write_b32 a7, v251\n\t" \
  "v_accvgpr_read_b32 v248, a8\n\t" \
  "v_accvgpr_read_b32 v249, a9\n\t" \
  "v_accvgpr_read_b32 v250, a10\n\t" \
  "v_accvgpr_read_b32 v251, a11\n\t" \
  "v_mul_f32 v248, v246, v248\n\t" \
  "v_mul_f32 v249, v246, v249\n\t" \
  "v_mul_f32 v250, v246, v250\n\t" \
  "v_mul_f32 v251, v246, v251\n\t" \
  "v_accvgpr_write_b32 a8, v248\n\t" \
  "v_accvgpr_write_b32 a9, v249\n\t" \
  "v_accvgpr_write_b32 a10, v250\n\t" \
  "v_accvgpr_write_b32 a11, v251\n\t" \
  "v_accvgpr_read_b32 v248, a12\n\t" \
  "v_accvgpr_read_b32 v249, a13\n\t" \
  "v_accvgpr_read_b32 v250, a14\n\t" \
  "v_accvgpr_read_b32 v251, a15\n\t" \
  "v_mul_f32 v248, v246, v248\n\t" \
  "v_mul_f32 v249, v246, v249\n\t" \
  "v_mul_f32 v250, v246, v250\n\t" \
  "v_mul_f32 v251, v246, v251\n\t" \
  "v_accvgpr_write_b32 a12, v248\n\t" \
  "v_accvgpr_write_b32 a13, v249\n\t" \
  "v_accvgpr_write_b32 a14, v250\n\t" \
  "v_accvgpr_write_b32 a15, v251\n\t" \
  "v_accvgpr_read_b32 v248, a16\n\t" \
  "v_accvgpr_read_b32 v249, a17\n\t" \
  "v_accvgpr_read_b32 v250, a18\n\t" \
  "v_accvgpr_read_b32 v251, a19\n\t" \
  "v_mul_f32 v248, v247, v248\n\t" \
  "v_mul_f32 v249, v247, v249\n\t" \
  "v_mul_f32 v250, v247, v250\n\t" \
  "v_mul_f32 v251, v247, v251\n\t" \
  "v_accvgpr_write_b32 a16, v248\n\t" \
  "v_accvgpr_write_b32 a17, v249\n\t" \
  "v_accvgpr_write_b32 a18, v250\n\t" \
  "v_accvgpr_write_b32 a19, v251\n\t" \
  "v_accvgpr_read_b32 v248, a20\n\t" \
  "v_accvgpr_read_b32 v249, a21\n\t" \
  "v_accvgpr_read_b32 v250, a22\n\t" \
  "v_accvgpr_read_b32 v251, a23\n\t" \
  "v_mul_f32 v248, v247, v248\n\t" \
  "v_mul_f32 v249, v247, v249\n\t" \
  "v_mul_f32 v250, v247, v250\n\t" \
  "v_mul_f32 v251, v247, v251\n\t" \
  "v_accvgpr_write_b32 a20, v248\n\t" \
  "v_accvgpr_write_b32 a21, v249\n\t" \
  "v_accvgpr_write_b32 a22, v250\n\t" \
  "v_accvgpr_write_b32 a23, v251\n\t" \
  "v_accvgpr_read_b32 v248, a24\n\t" \
  "v_accvgpr_read_b32 v249, a25\n\t" \
  "v_accvgpr_read_b32 v250, a26\n\t" \
  "v_accvgpr_read_b32 v251, a27\n\t" \
  "v_mul_f32 v248, v247, v248\n\t" \
  "v_mul_f32 v249, v247, v249\n\t" \
  "v_mul_f32 v250, v247, v250\n\t" \
  "v_mul_f32 v251, v247, v251\n\t" \
  "v_accvgpr_write_b32 a24, v248\n\t" \
  "v_accvgpr_write_b32 a25, v249\n\t" \
  "v_accvgpr_write_b32 a26, v250\n\t" \
  "v_accvgpr_write_b32 a27, v251\n\t" \
  "v_accvgpr_read_b32 v248, a28\n\t" \
  "v_accvgpr_read_b32 v249, a29\n\t" \
  "v_accvgpr_read_b32 v250, a30\n\t" \
  "v_accvgpr_read_b32 v251, a31\n\t" \
  "v_mul_f32 v248, v247, v248\n\t" \
  "v_mul_f32 v249, v247, v249\n\t" \
  "v_mul_f32 v250, v247, v250\n\t" \
  "v_mul_f32 v251, v247, v251\n\t" \
  "v_accvgpr_write_b32 a28, v248\n\t" \
  "v_accvgpr_write_b32 a29, v249\n\t" \
  "v_accvgpr_write_b32 a30, v250\n\t" \
  "v_accvgpr_write_b32 a31, v251\n\t" \
  "v_accvgpr_read_b32 v248, a32\n\t" \
  "v_accvgpr_read_b32 v249, a33\n\t" \
  "v_accvgpr_read_b32 v250, a34\n\t" \
  "v_accvgpr_read_b32 v251, a35\n\t" \
  "v_mul_f32 v248, v246, v248\n\t" \
  "v_mul_f32 v249, v246, v249\n\t" \
  "v_mul_f32 v250, v246, v250\n\t" \
  "v_mul_f32 v251, v246, v251\n\t" \
  "v_accvgpr_write_b32 a32, v248\n\t" \
  "v_accvgpr_write_b32 a33, v249\n\t" \
  "v_accvgpr_write_b32 a34, v250\n\t" \
  "v_accvgpr_write_b32 a35, v251\n\t" \
  "v_accvgpr_read_b32 v248, a36\n\t" \
  "v_accvgpr_read_b32 v249, a37\n\t" \
  "v_accvgpr_read_b32 v250, a38\n\t" \
  "v_accvgpr_read_b32 v251, a39\n\t" \
  "v_mul_f32 v248, v246, v248\n\t" \
  "v_mul_f32 v249, v246, v249\n\t" \
  "v_mul_f32 v250, v246, v250\n\t" \
  "v_mul_f32 v251, v246, v251\n\t" \
  "v_accvgpr_write_b32 a36, v248\n\t" \
  "v_accvgpr_write_b32 a37, v249\n\t" \
  "v_accvgpr_write_b32 a38, v250\n\t" \
  "v_accvgpr_write_b32 a39, v251\n\t" \
  "v_accvgpr_read_b32 v248, a40\n\t" \
  "v_accvgpr_read_b32 v249, a41\n\t" \
  "v_accvgpr_read_b32 v250, a42\n\t" \
  "v_accvgpr_read_b32 v251, a43\n\t" \
  "v_mul_f32 v248, v246, v248\n\t" \
  "v_mul_f32 v249, v246, v249\n\t" \
  "v_mul_f32 v250, v246, v250\n\t" \
  "v_mul_f32 v251, v246, v251\n\t" \
  "v_accvgpr_write_b32 a40, v248\n\t" \
  "v_accvgpr_write_b32 a41, v249\n\t" \
  "v_accvgpr_write_b32 a42, v250\n\t" \
  "v_accvgpr_write_b32 a43, v251\n\t" \
  "v_accvgpr_read_b32 v248, a44\n\t" \
  "v_accvgpr_read_b32 v249, a45\n\t" \
  "v_accvgpr_read_b32 v250, a46\n\t" \
  "v_accvgpr_read_b32 v251, a47\n\t" \
  "v_mul_f32 v248, v246, v248\n\t" \
  "v_mul_f32 v249, v246, v249\n\t" \
  "v_mul_f32 v250, v246, v250\n\t" \
  "v_mul_f32 v251, v246, v251\n\t" \
  "v_accvgpr_write_b32 a44, v248\n\t" \
  "v_accvgpr_write_b32 a45, v249\n\t" \
  "v_accvgpr_write_b32 a46, v250\n\t" \
  "v_accvgpr_write_b32 a47, v251\n\t" \
  "v_accvgpr_read_b32 v248, a48\n\t" \
  "v_accvgpr_read_b32 v249, a49\n\t" \
  "v_accvgpr_read_b32 v250, a50\n\t" \
  "v_accvgpr_read_b32 v251, a51\n\t" \
  "v_mul_f32 v248, v247, v248\n\t" \
  "v_mul_f32 v249, v247, v249\n\t" \
  "v_mul_f32 v250, v247, v250\n\t" \
  "v_mul_f32 v251, v247, v251\n\t" \
  "v_accvgpr_write_b32 a48, v248\n\t" \
  "v_accvgpr_write_b32 a49, v249\n\t" \
  "v_accvgpr_write_b32 a50, v250\n\t" \
  "v_accvgpr_write_b32 a51, v251\n\t" \
  "v_accvgpr_read_b32 v248, a52\n\t" \
  "v_accvgpr_read_b32 v249, a53\n\t" \
  "v_accvgpr_read_b32 v250, a54\n\t" \
  "v_accvgpr_read_b32 v251, a55\n\t" \
  "v_mul_f32 v248, v247, v248\n\t" \
  "v_mul_f32 v249, v247, v249\n\t" \
  "v_mul_f32 v250, v247, v250\n\t" \
  "v_mul_f32 v251, v247, v251\n\t" \
  "v_accvgpr_write_b32 a52, v248\n\t" \
  "v_accvgpr_write_b32 a53, v249\n\t" \
  "v_accvgpr_write_b32 a54, v250\n\t" \
  "v_accvgpr_write_b32 a55, v251\n\t" \
  "v_accvgpr_read_b32 v248, a56\n\t" \
  "v_accvgpr_read_b32 v249, a57\n\t" \
  "v_accvgpr_read_b32 v250, a58\n\t" \
  "v_accvgpr_read_b32 v251, a59\n\t" \
  "v_mul_f32 v248, v247, v248\n\t" \
  "v_mul_f32 v249, v247, v249\n\t" \
  "v_mul_f32 v250, v247, v250\n\t" \
  "v_mul_f32 v251, v247, v251\n\t" \
  "v_accvgpr_write_b32 a56, v248\n\t" \
  "v_accvgpr_write_b32 a57, v249\n\t" \
  "v_accvgpr_write_b32 a58, v250\n\t" \
  "v_accvgpr_write_b32 a59, v251\n\t" \
  "v_accvgpr_read_b32 v248, a60\n\t" \
  "v_accvgpr_read_b32 v249, a61\n\t" \
  "v_accvgpr_read_b32 v250, a62\n\t" \
  "v_accvgpr_read_b32 v251, a63\n\t" \
  "v_mul_f32 v248, v247, v248\n\t" \
  "v_mul_f32 v249, v247, v249\n\t" \
  "v_mul_f32 v250, v247, v250\n\t" \
  "v_mul_f32 v251, v247, v251\n\t" \
  "v_accvgpr_write_b32 a60, v248\n\t" \
  "v_accvgpr_write_b32 a61, v249\n\t" \
  "v_accvgpr_write_b32 a62, v250\n\t" \
  "v_accvgpr_write_b32 a63, v251\n\t" \
  "v_accvgpr_read_b32 v248, a64\n\t" \
  "v_accvgpr_read_b32 v249, a65\n\t" \
  "v_accvgpr_read_b32 v250, a66\n\t" \
  "v_accvgpr_read_b32 v251, a67\n\t" \
  "v_mul_f32 v248, v246, v248\n\t" \
  "v_mul_f32 v249, v246, v249\n\t" \
  "v_mul_f32 v250, v246, v250\n\t" \
  "v_mul_f32 v251, v246, v251\n\t" \
  "v_accvgpr_write_b32 a64, v248\n\t" \
  "v_accvgpr_write_b32 a65, v249\n\t" \
  "v_accvgpr_write_b32 a66, v250\n\t" \
  "v_accvgpr_write_b32 a67, v251\n\t" \
  "v_accvgpr_read_b32 v248, a68\n\t" \
  "v_accvgpr_read_b32 v249, a69\n\t" \
  "v_accvgpr_read_b32 v250, a70\n\t" \
  "v_accvgpr_read_b32 v251, a71\n\t" \
  "v_mul_f32 v248, v246, v248\n\t" \
  "v_mul_f32 v249, v246, v249\n\t" \
  "v_mul_f32 v250, v246, v250\n\t" \
  "v_mul_f32 v251, v246, v251\n\t" \
  "v_accvgpr_write_b32 a68, v248\n\t" \
  "v_accvgpr_write_b32 a69, v249\n\t" \
  "v_accvgpr_write_b32 a70, v250\n\t" \
  "v_accvgpr_write_b32 a71, v251\n\t" \
  "v_accvgpr_read_b32 v248, a72\n\t" \
  "v_accvgpr_read_b32 v249, a73\n\t" \
  "v_accvgpr_read_b32 v250, a74\n\t" \
  "v_accvgpr_read_b32 v251, a75\n\t" \
  "v_mul_f32 v248, v246, v248\n\t" \
  "v_mul_f32 v249, v246, v249\n\t" \
  "v_mul_f32 v250, v246, v250\n\t" \
  "v_mul_f32 v251, v246, v251\n\t" \
  "v_accvgpr_write_b32 a72, v248\n\t" \
  "v_accvgpr_write_b32 a73, v249\n\t" \
  "v_accvgpr_write_b32 a74, v250\n\t" \
  "v_accvgpr_write_b32 a75, v251\n\t" \
  "v_accvgpr_read_b32 v248, a76\n\t" \
  "v_accvgpr_read_b32 v249, a77\n\t" \
  "v_accvgpr_read_b32 v250, a78\n\t" \
  "v_accvgpr_read_b32 v251, a79\n\t" \
  "v_mul_f32 v248, v246, v248\n\t" \
  "v_mul_f32 v249, v246, v249\n\t" \
  "v_mul_f32 v250, v246, v250\n\t" \
  "v_mul_f32 v251, v246, v251\n\t" \
  "v_accvgpr_write_b32 a76, v248\n\t" \
  "v_accvgpr_write_b32 a77, v249\n\t" \
  "v_accvgpr_write_b32 a78, v250\n\t" \
  "v_accvgpr_write_b32 a79, v251\n\t" \
  "v_accvgpr_read_b32 v248, a80\n\t" \
  "v_accvgpr_read_b32 v249, a81\n\t" \
  "v_accvgpr_read_b32 v250, a82\n\t" \
  "v_accvgpr_read_b32 v251, a83\n\t" \
  "v_mul_f32 v248, v247, v248\n\t" \
  "v_mul_f32 v249, v247, v249\n\t" \
  "v_mul_f32 v250, v247, v250\n\t" \
  "v_mul_f32 v251, v247, v251\n\t" \
  "v_accvgpr_write_b32 a80, v248\n\t" \
  "v_accvgpr_write_b32 a81, v249\n\t" \
  "v_accvgpr_write_b32 a82, v250\n\t" \
  "v_accvgpr_write_b32 a83, v251\n\t" \
  "v_accvgpr_read_b32 v248, a84\n\t" \
  "v_accvgpr_read_b32 v249, a85\n\t" \
  "v_accvgpr_read_b32 v250, a86\n\t" \
  "v_accvgpr_read_b32 v251, a87\n\t" \
  "v_mul_f32 v248, v247, v248\n\t" \
  "v_mul_f32 v249, v247, v249\n\t" \
  "v_mul_f32 v250, v247, v250\n\t" \
  "v_mul_f32 v251, v247, v251\n\t" \
  "v_accvgpr_write_b32 a84, v248\n\t" \
  "v_accvgpr_write_b32 a85, v249\n\t" \
  "v_accvgpr_write_b32 a86, v250\n\t" \
  "v_accvgpr_write_b32 a87, v251\n\t" \
  "v_accvgpr_read_b32 v248, a88\n\t" \
  "v_accvgpr_read_b32 v249, a89\n\t" \
  "v_accvgpr_read_b32 v250, a90\n\t" \
  "v_accvgpr_read_b32 v251, a91\n\t" \
  "v_mul_f32 v248, v247, v248\n\t" \
  "v_mul_f32 v249, v247, v249\n\t" \
  "v_mul_f32 v250, v247, v250\n\t" \
  "v_mul_f32 v251, v247, v251\n\t" \
  "v_accvgpr_write_b32 a88, v248\n\t" \
  "v_accvgpr_write_b32 a89, v249\n\t" \
  "v_accvgpr_write_b32 a90, v250\n\t" \
  "v_accvgpr_write_b32 a91, v251\n\t" \
  "v_accvgpr_read_b32 v248, a92\n\t" \
  "v_accvgpr_read_b32 v249, a93\n\t" \
  "v_accvgpr_read_b32 v250, a94\n\t" \
  "v_accvgpr_read_b32 v251, a95\n\t" \
  "v_mul_f32 v248, v247, v248\n\t" \
  "v_mul_f32 v249, v247, v249\n\t" \
  "v_mul_f32 v250, v247, v250\n\t" \
  "v_mul_f32 v251, v247, v251\n\t" \
  "v_accvgpr_write_b32 a92, v248\n\t" \
  "v_accvgpr_write_b32 a93, v249\n\t" \
  "v_accvgpr_write_b32 a94, v250\n\t" \
  "v_accvgpr_write_b32 a95, v251\n\t" \
  "v_accvgpr_read_b32 v248, a96\n\t" \
  "v_accvgpr_read_b32 v249, a97\n\t" \
  "v_accvgpr_read_b32 v250, a98\n\t" \
  "v_accvgpr_read_b32 v251, a99\n\t" \
  "v_mul_f32 v248, v246, v248\n\t" \
  "v_mul_f32 v249, v246, v249\n\t" \
  "v_mul_f32 v250, v246, v250\n\t" \
  "v_mul_f32 v251, v246, v251\n\t" \
  "v_accvgpr_write_b32 a96, v248\n\t" \
  "v_accvgpr_write_b32 a97, v249\n\t" \
  "v_accvgpr_write_b32 a98, v250\n\t" \
  "v_accvgpr_write_b32 a99, v251\n\t" \
  "v_accvgpr_read_b32 v248, a100\n\t" \
  "v_accvgpr_read_b32 v249, a101\n\t" \
  "v_accvgpr_read_b32 v250, a102\n\t" \
  "v_accvgpr_read_b32 v251, a103\n\t" \
  "v_mul_f32 v248, v246, v248\n\t" \
  "v_mul_f32 v249, v246, v249\n\t" \
  "v_mul_f32 v250, v246, v250\n\t" \
  "v_mul_f32 v251, v246, v251\n\t" \
  "v_accvgpr_write_b32 a100, v248\n\t" \
  "v_accvgpr_write_b32 a101, v249\n\t" \
  "v_accvgpr_write_b32 a102, v250\n\t" \
  "v_accvgpr_write_b32 a103, v251\n\t" \
  "v_accvgpr_read_b32 v248, a104\n\t" \
  "v_accvgpr_read_b32 v249, a105\n\t" \
  "v_accvgpr_read_b32 v250, a106\n\t" \
  "v_accvgpr_read_b32 v251, a107\n\t" \
  "v_mul_f32 v248, v246, v248\n\t" \
  "v_mul_f32 v249, v246, v249\n\t" \
  "v_mul_f32 v250, v246, v250\n\t" \
  "v_mul_f32 v251, v246, v251\n\t" \
  "v_accvgpr_write_b32 a104, v248\n\t" \
  "v_accvgpr_write_b32 a105, v249\n\t" \
  "v_accvgpr_write_b32 a106, v250\n\t" \
  "v_accvgpr_write_b32 a107, v251\n\t" \
  "v_accvgpr_read_b32 v248, a108\n\t" \
  "v_accvgpr_read_b32 v249, a109\n\t" \
  "v_accvgpr_read_b32 v250, a110\n\t" \
  "v_accvgpr_read_b32 v251, a111\n\t" \
  "v_mul_f32 v248, v246, v248\n\t" \
  "v_mul_f32 v249, v246, v249\n\t" \
  "v_mul_f32 v250, v246, v250\n\t" \
  "v_mul_f32 v251, v246, v251\n\t" \
  "v_accvgpr_write_b32 a108, v248\n\t" \
  "v_accvgpr_write_b32 a109, v249\n\t" \
  "v_accvgpr_write_b32 a110, v250\n\t" \
  "v_accvgpr_write_b32 a111, v251\n\t" \
  "v_accvgpr_read_b32 v248, a112\n\t" \
  "v_accvgpr_read_b32 v249, a113\n\t" \
  "v_accvgpr_read_b32 v250, a114\n\t" \
  "v_accvgpr_read_b32 v251, a115\n\t" \
  "v_mul_f32 v248, v247, v248\n\t" \
  "v_mul_f32 v249, v247, v249\n\t" \
  "v_mul_f32 v250, v247, v250\n\t" \
  "v_mul_f32 v251, v247, v251\n\t" \
  "v_accvgpr_write_b32 a112, v248\n\t" \
  "v_accvgpr_write_b32 a113, v249\n\t" \
  "v_accvgpr_write_b32 a114, v250\n\t" \
  "v_accvgpr_write_b32 a115, v251\n\t" \
  "v_accvgpr_read_b32 v248, a116\n\t" \
  "v_accvgpr_read_b32 v249, a117\n\t" \
  "v_accvgpr_read_b32 v250, a118\n\t" \
  "v_accvgpr_read_b32 v251, a119\n\t" \
  "v_mul_f32 v248, v247, v248\n\t" \
  "v_mul_f32 v249, v247, v249\n\t" \
  "v_mul_f32 v250, v247, v250\n\t" \
  "v_mul_f32 v251, v247, v251\n\t" \
  "v_accvgpr_write_b32 a116, v248\n\t" \
  "v_accvgpr_write_b32 a117, v249\n\t" \
  "v_accvgpr_write_b32 a118, v250\n\t" \
  "v_accvgpr_write_b32 a119, v251\n\t" \
  "v_accvgpr_read_b32 v248, a120\n\t" \
  "v_accvgpr_read_b32 v249, a121\n\t" \
  "v_accvgpr_read_b32 v250, a122\n\t" \
  "v_accvgpr_read_b32 v251, a123\n\t" \
  "v_mul_f32 v248, v247, v248\n\t" \
  "v_mul_f32 v249, v247, v249\n\t" \
  "v_mul_f32 v250, v247, v250\n\t" \
  "v_mul_f32 v251, v247, v251\n\t" \
  "v_accvgpr_write_b32 a120, v248\n\t" \
  "v_accvgpr_write_b32 a121, v249\n\t" \
  "v_accvgpr_write_b32 a122, v250\n\t" \
  "v_accvgpr_write_b32 a123, v251\n\t" \
  "v_accvgpr_read_b32 v248, a124\n\t" \
  "v_accvgpr_read_b32 v249, a125\n\t" \
  "v_accvgpr_read_b32 v250, a126\n\t" \
  "v_accvgpr_read_b32 v251, a127\n\t" \
  "v_mul_f32 v248, v247, v248\n\t" \
  "v_mul_f32 v249, v247, v249\n\t" \
  "v_mul_f32 v250, v247, v250\n\t" \
  "v_mul_f32 v251, v247, v251\n\t" \
  "v_accvgpr_write_b32 a124, v248\n\t" \
  "v_accvgpr_write_b32 a125, v249\n\t" \
  "v_accvgpr_write_b32 a126, v250\n\t" \
  "v_accvgpr_write_b32 a127, v251\n\t" \
  "s_nop 1\n\t" \
  "s_branch .Lx0_%=_RBR\n\t" \
  ".LDONE%=:\n\t" \
  "ds_read_b64_tr_b16 v[128:129], v228 offset:16384\n\t" \
  "v_exp_f32 v96, v96\n\t" \
  "v_exp_f32 v97, v97\n\t" \
  "v_cvt_pk_bf16_f32 v64, v64, v65\n\t" \
  "ds_read_b64_tr_b16 v[130:131], v228 offset:18432\n\t" \
  "v_exp_f32 v98, v98\n\t" \
  "v_exp_f32 v99, v99\n\t" \
  "v_cvt_pk_bf16_f32 v65, v66, v67\n\t" \
  "ds_read_b64_tr_b16 v[132:133], v228 offset:16896\n\t" \
  "v_exp_f32 v100, v100\n\t" \
  "v_exp_f32 v101, v101\n\t" \
  "v_cvt_pk_bf16_f32 v66, v68, v69\n\t" \
  "ds_read_b64_tr_b16 v[134:135], v228 offset:18944\n\t" \
  "v_exp_f32 v102, v102\n\t" \
  "v_exp_f32 v103, v103\n\t" \
  "v_cvt_pk_bf16_f32 v67, v70, v71\n\t" \
  "ds_read_b64_tr_b16 v[136:137], v228 offset:17408\n\t" \
  "v_add_f32 v232, v232, v68\n\t" \
  "v_exp_f32 v104, v104\n\t" \
  "v_cvt_pk_bf16_f32 v72, v72, v73\n\t" \
  "v_add_f32 v232, v232, v96\n\t" \
  "ds_read_b64_tr_b16 v[138:139], v228 offset:19456\n\t" \
  "v_add_f32 v233, v233, v69\n\t" \
  "v_exp_f32 v105, v105\n\t" \
  "v_cvt_pk_bf16_f32 v73, v74, v75\n\t" \
  "v_add_f32 v233, v233, v97\n\t" \
  "ds_read_b64_tr_b16 v[140:141], v228 offset:17920\n\t" \
  "v_add_f32 v232, v232, v70\n\t" \
  "v_exp_f32 v106, v106\n\t" \
  "v_cvt_pk_bf16_f32 v74, v76, v77\n\t" \
  "v_add_f32 v232, v232, v98\n\t" \
  "ds_read_b64_tr_b16 v[142:143], v228 offset:19968\n\t" \
  "v_add_f32 v233, v233, v71\n\t" \
  "v_exp_f32 v107, v107\n\t" \
  "v_cvt_pk_bf16_f32 v75, v78, v79\n\t" \
  "v_add_f32 v233, v233, v99\n\t" \
  "ds_read_b64_tr_b16 v[144:145], v228 offset:20480\n\t" \
  "v_add_f32 v232, v232, v76\n\t" \
  "v_exp_f32 v108, v108\n\t" \
  "v_cvt_pk_bf16_f32 v80, v80, v81\n\t" \
  "v_add_f32 v232, v232, v100\n\t" \
  "ds_read_b64_tr_b16 v[146:147], v228 offset:22528\n\t" \
  "v_add_f32 v233, v233, v77\n\t" \
  "v_exp_f32 v109, v109\n\t" \
  "v_cvt_pk_bf16_f32 v81, v82, v83\n\t" \
  "v_add_f32 v233, v233, v101\n\t" \
  "ds_read_b64_tr_b16 v[148:149], v228 offset:20992\n\t" \
  "v_add_f32 v232, v232, v78\n\t" \
  "v_exp_f32 v110, v110\n\t" \
  "v_cvt_pk_bf16_f32 v82, v84, v85\n\t" \
  "v_add_f32 v232, v232, v102\n\t" \
  "ds_read_b64_tr_b16 v[150:151], v228 offset:23040\n\t" \
  "v_add_f32 v233, v233, v79\n\t" \
  "v_exp_f32 v111, v111\n\t" \
  "v_cvt_pk_bf16_f32 v83, v86, v87\n\t" \
  "v_add_f32 v233, v233, v103\n\t" \
  "ds_read_b64_tr_b16 v[152:153], v228 offset:21504\n\t" \
  "v_add_f32 v234, v234, v84\n\t" \
  "v_exp_f32 v112, v112\n\t" \
  "v_cvt_pk_bf16_f32 v88, v88, v89\n\t" \
  "v_add_f32 v232, v232, v104\n\t" \
  "ds_read_b64_tr_b16 v[154:155], v228 offset:23552\n\t" \
  "v_add_f32 v235, v235, v85\n\t" \
  "v_exp_f32 v113, v113\n\t" \
  "v_cvt_pk_bf16_f32 v89, v90, v91\n\t" \
  "v_add_f32 v233, v233, v105\n\t" \
  "ds_read_b64_tr_b16 v[156:157], v228 offset:22016\n\t" \
  "v_add_f32 v234, v234, v86\n\t" \
  "v_exp_f32 v114, v114\n\t" \
  "v_cvt_pk_bf16_f32 v90, v92, v93\n\t" \
  "v_add_f32 v232, v232, v106\n\t" \
  "ds_read_b64_tr_b16 v[158:159], v228 offset:24064\n\t" \
  "v_add_f32 v235, v235, v87\n\t" \
  "v_exp_f32 v115, v115\n\t" \
  "v_cvt_pk_bf16_f32 v91, v94, v95\n\t" \
  "v_add_f32 v233, v233, v107\n\t" \
  "ds_read_b64_tr_b16 v[160:161], v228 offset:24576\n\t" \
  "v_add_f32 v234, v234, v92\n\t" \
  "v_exp_f32 v116, v116\n\t" \
  "v_add_f32 v232, v232, v108\n\t" \
  "v_cvt_pk_bf16_f32 v68, v96, v97\n\t" \
  "ds_read_b64_tr_b16 v[162:163], v228 offset:26624\n\t" \
  "v_add_f32 v235, v235, v93\n\t" \
  "v_exp_f32 v117, v117\n\t" \
  "v_add_f32 v233, v233, v109\n\t" \
  "v_cvt_pk_bf16_f32 v69, v98, v99\n\t" \
  "ds_read_b64_tr_b16 v[164:165], v228 offset:25088\n\t" \
  "v_add_f32 v234, v234, v94\n\t" \
  "v_exp_f32 v118, v118\n\t" \
  "v_add_f32 v232, v232, v110\n\t" \
  "v_cvt_pk_bf16_f32 v70, v100, v101\n\t" \
  "ds_read_b64_tr_b16 v[166:167], v228 offset:27136\n\t" \
  "v_add_f32 v235, v235, v95\n\t" \
  "v_exp_f32 v119, v119\n\t" \
  "v_add_f32 v233, v233, v111\n\t" \
  "v_cvt_pk_bf16_f32 v71, v102, v103\n\t" \
  "ds_read_b64_tr_b16 v[170:171], v228 offset:27648\n\t" \
  "ds_read_b64_tr_b16 v[168:169], v228 offset:25600\n\t" \
  "v_exp_f32 v120, v120\n\t" \
  "v_add_f32 v234, v234, v112\n\t" \
  "v_cvt_pk_bf16_f32 v76, v104, v105\n\t" \
  "ds_read_b64_tr_b16 v[174:175], v228 offset:28160\n\t" \
  "ds_read_b64_tr_b16 v[172:173], v228 offset:26112\n\t" \
  "v_exp_f32 v121, v121\n\t" \
  "v_add_f32 v235, v235, v113\n\t" \
  "v_cvt_pk_bf16_f32 v77, v106, v107\n\t" \
  "ds_read_b64_tr_b16 v[178:179], v228 offset:30720\n\t" \
  "ds_read_b64_tr_b16 v[176:177], v228 offset:28672\n\t" \
  "v_exp_f32 v122, v122\n\t" \
  "v_add_f32 v234, v234, v114\n\t" \
  "v_cvt_pk_bf16_f32 v78, v108, v109\n\t" \
  "ds_read_b64_tr_b16 v[182:183], v228 offset:31232\n\t" \
  "ds_read_b64_tr_b16 v[180:181], v228 offset:29184\n\t" \
  "v_exp_f32 v123, v123\n\t" \
  "v_add_f32 v235, v235, v115\n\t" \
  "v_cvt_pk_bf16_f32 v79, v110, v111\n\t" \
  "ds_read_b64_tr_b16 v[186:187], v228 offset:31744\n\t" \
  "ds_read_b64_tr_b16 v[184:185], v228 offset:29696\n\t" \
  "v_exp_f32 v124, v124\n\t" \
  "v_add_f32 v234, v234, v116\n\t" \
  "v_cvt_pk_bf16_f32 v84, v112, v113\n\t" \
  "ds_read_b64_tr_b16 v[190:191], v228 offset:32256\n\t" \
  "ds_read_b64_tr_b16 v[188:189], v228 offset:30208\n\t" \
  "v_exp_f32 v125, v125\n\t" \
  "v_add_f32 v235, v235, v117\n\t" \
  "v_cvt_pk_bf16_f32 v85, v114, v115\n\t" \
  "v_exp_f32 v126, v126\n\t" \
  "v_add_f32 v234, v234, v118\n\t" \
  "v_cvt_pk_bf16_f32 v86, v116, v117\n\t" \
  "v_exp_f32 v127, v127\n\t" \
  "v_add_f32 v235, v235, v119\n\t" \
  "v_cvt_pk_bf16_f32 v87, v118, v119\n\t" \
  "v_add_f32 v234, v234, v120\n\t" \
  "v_add_f32 v235, v235, v121\n\t" \
  "v_cvt_pk_bf16_f32 v92, v120, v121\n\t" \
  "v_add_f32 v234, v234, v122\n\t" \
  "v_add_f32 v235, v235, v123\n\t" \
  "v_cvt_pk_bf16_f32 v93, v122, v123\n\t" \
  "v_add_f32 v234, v234, v124\n\t" \
  "v_add_f32 v235, v235, v125\n\t" \
  "v_cvt_pk_bf16_f32 v94, v124, v125\n\t" \
  "v_add_f32 v234, v234, v126\n\t" \
  "v_add_f32 v235, v235, v127\n\t" \
  "v_cvt_pk_bf16_f32 v95, v126, v127\n\t" \
  "s_nop 0\n\t" \
  "v_add_f32 v248, v232, v233\n\t" \
  "v_add_f32 v249, v234, v235\n\t" \
  "v_mov_b32 v250, v248\n\t" \
  "v_mov_b32 v251, v249\n\t" \
  "s_nop 1\n\t" \
  "v_permlane32_swap_b32 v248, v250\n\t" \
  "v_permlane32_swap_b32 v249, v251\n\t" \
  "v_add_f32 v248, v248, v250\n\t" \
  "v_add_f32 v249, v249, v251\n\t" \
  "v_mov_b32 v238, v248\n\t" \
  "v_mov_b32 v239, v249\n\t" \
  "s_waitcnt lgkmcnt(0)\n\t" \
  "s_nop 1\n\t" \
  "v_mfma_f32_32x32x16_bf16 a[0:15], v[128:131], v[64:67], a[0:15]\n\t" \
  "v_mfma_f32_32x32x16_bf16 a[16:31], v[128:131], v[80:83], a[16:31]\n\t" \
  "v_mfma_f32_32x32x16_bf16 a[32:47], v[132:135], v[64:67], a[32:47]\n\t" \
  "v_mfma_f32_32x32x16_bf16 a[48:63], v[132:135], v[80:83], a[48:63]\n\t" \
  "v_mfma_f32_32x32x16_bf16 a[64:79], v[136:139], v[64:67], a[64:79]\n\t" \
  "v_mfma_f32_32x32x16_bf16 a[80:95], v[136:139], v[80:83], a[80:95]\n\t" \
  "v_mfma_f32_32x32x16_bf16 a[96:111], v[140:143], v[64:67], a[96:111]\n\t" \
  "v_mfma_f32_32x32x16_bf16 a[112:127], v[140:143], v[80:83], a[112:127]\n\t" \
  "v_mfma_f32_32x32x16_bf16 a[0:15], v[144:147], v[72:75], a[0:15]\n\t" \
  "v_mfma_f32_32x32x16_bf16 a[16:31], v[144:147], v[88:91], a[16:31]\n\t" \
  "v_mfma_f32_32x32x16_bf16 a[32:47], v[148:151], v[72:75], a[32:47]\n\t" \
  "v_mfma_f32_32x32x16_bf16 a[48:63], v[148:151], v[88:91], a[48:63]\n\t" \
  "v_mfma_f32_32x32x16_bf16 a[64:79], v[152:155], v[72:75], a[64:79]\n\t" \
  "v_mfma_f32_32x32x16_bf16 a[80:95], v[152:155], v[88:91], a[80:95]\n\t" \
  "v_mfma_f32_32x32x16_bf16 a[96:111], v[156:159], v[72:75], a[96:111]\n\t" \
  "v_mfma_f32_32x32x16_bf16 a[112:127], v[156:159], v[88:91], a[112:127]\n\t" \
  "v_mfma_f32_32x32x16_bf16 a[0:15], v[160:163], v[68:71], a[0:15]\n\t" \
  "v_mfma_f32_32x32x16_bf16 a[16:31], v[160:163], v[84:87], a[16:31]\n\t" \
  "v_mfma_f32_32x32x16_bf16 a[32:47], v[164:167], v[68:71], a[32:47]\n\t" \
  "v_mfma_f32_32x32x16_bf16 a[48:63], v[164:167], v[84:87], a[48:63]\n\t" \
  "v_mfma_f32_32x32x16_bf16 a[64:79], v[168:171], v[68:71], a[64:79]\n\t" \
  "v_mfma_f32_32x32x16_bf16 a[80:95], v[168:171], v[84:87], a[80:95]\n\t" \
  "v_mfma_f32_32x32x16_bf16 a[96:111], v[172:175], v[68:71], a[96:111]\n\t" \
  "v_mfma_f32_32x32x16_bf16 a[112:127], v[172:175], v[84:87], a[112:127]\n\t" \
  "v_mfma_f32_32x32x16_bf16 a[0:15], v[176:179], v[76:79], a[0:15]\n\t" \
  "v_mfma_f32_32x32x16_bf16 a[16:31], v[176:179], v[92:95], a[16:31]\n\t" \
  "v_mfma_f32_32x32x16_bf16 a[32:47], v[180:183], v[76:79], a[32:47]\n\t" \
  "v_mfma_f32_32x32x16_bf16 a[48:63], v[180:183], v[92:95], a[48:63]\n\t" \
  "v_mfma_f32_32x32x16_bf16 a[64:79], v[184:187], v[76:79], a[64:79]\n\t" \
  "v_mfma_f32_32x32x16_bf16 a[80:95], v[184:187], v[92:95], a[80:95]\n\t" \
  "v_mfma_f32_32x32x16_bf16 a[96:111], v[188:191], v[76:79], a[96:111]\n\t" \
  "v_mfma_f32_32x32x16_bf16 a[112:127], v[188:191], v[92:95], a[112:127]\n\t" \
  "s_nop 7\n\t" \
  "s_nop 7\n\t"
#define ATTN_CORE_VCLOB "v0", "v1", "v2", "v3", "v4", "v5", "v6", "v7", "v8", "v9", "v10", "v11", "v12", "v13", "v14", "v15", "v16", "v17", "v18", "v19", "v20", "v21", "v22", "v23", "v24", "v25", "v26", "v27", "v28", "v29", "v30", "v31", "v32", "v33", "v34", "v35", "v36", "v37", "v38", "v39", "v40", "v41", "v42", "v43", "v44", "v45", "v46", "v47", "v48", "v49", "v50", "v51", "v52", "v53", "v54", "v55", "v56", "v57", "v58", "v59", "v60", "v61", "v62", "v63", "v64", "v65", "v66", "v67", "v68", "v69", "v70", "v71", "v72", "v73", "v74", "v75", "v76", "v77", "v78", "v79", "v80", "v81", "v82", "v83", "v84", "v85", "v86", "v87", "v88", "v89", "v90", "v91", "v92", "v93", "v94", "v95", "v96", "v97", "v98", "v99", "v100", "v101", "v102", "v103", "v104", "v105", "v106", "v107", "v108", "v109", "v110", "v111", "v112", "v113", "v114", "v115", "v116", "v117", "v118", "v119", "v120", "v121", "v122", "v123", "v124", "v125", "v126", "v127", "v128", "v129", "v130", "v131", "v132", "v133", "v134", "v135", "v136", "v137", "v138", "v139", "v140", "v141", "v142", "v143", "v144", "v145", "v146", "v147", "v148", "v149", "v150", "v151", "v152", "v153", "v154", "v155", "v156", "v157", "v158", "v159", "v160", "v161", "v162", "v163", "v164", "v165", "v166", "v167", "v168", "v169", "v170", "v171", "v172", "v173", "v174", "v175", "v176", "v177", "v178", "v179", "v180", "v181", "v182", "v183", "v184", "v185", "v186", "v187", "v188", "v189", "v190", "v191", "v192", "v193", "v194", "v195", "v196", "v197", "v198", "v199", "v200", "v201", "v202", "v203", "v204", "v205", "v206", "v207", "v208", "v209", "v210", "v211", "v212", "v213", "v214", "v215", "v216", "v217", "v218", "v219", "v220", "v221", "v222", "v223", "v240", "v241", "v242", "v243", "v244", "v245", "v246", "v247", "v248", "v249", "v250", "v251"

template <int DB, int QB, bool ZERO> DI void qp_mfma(const bf16x8& a, const bf16x8& b) {
  constexpr int ao = AO_(DB, QB);
  if constexpr (ZERO) asm volatile("v_mfma_f32_32x32x16_bf16 a[%c2:%c3], %0, %1, 0" :: "v"(a), "v"(b), "i"(ao), "i"(ao + 15) : W4_ACLOB);
  else                asm volatile("v_mfma_f32_32x32x16_bf16 a[%c2:%c3], %0, %1, a[%c2:%c3]" :: "v"(a), "v"(b), "i"(ao), "i"(ao + 15) : W4_ACLOB);
}
typedef short qs16x4 __attribute__((ext_vector_type(4)));
typedef __attribute__((address_space(3))) qs16x4* qlds_p;
#define POST_CODE_BYTES 32768
#define QP_SLOT 49152u
#define QP_AUX 147456u
#define QP_STAT 151552u
DI void qdma(unsigned m0v, unsigned voff, __amdgpu_buffer_rsrc_t srd, unsigned soff) {
  asm volatile("s_mov_b32 m0, %0\n\ts_nop 3\n\tbuffer_load_dwordx4 %1, %2, %3 offen lds" :: "s"(m0v), "v"(voff), "s"(srd), "s"(soff) : "m0", "memory");
}
extern "C" __global__ __launch_bounds__(256, 1)
void attn_kernel(const unsigned short* __restrict__ Xb, const float* __restrict__ modf, const unsigned short* __restrict__ Wq,
                 const unsigned short* __restrict__ Kb, const unsigned short* __restrict__ Vb, unsigned short* __restrict__ O,
                 const unsigned short* __restrict__ Wo, const unsigned short* __restrict__ Wg, const unsigned short* __restrict__ Wu, const unsigned short* __restrict__ Wd
) {
  extern __shared__ __attribute__((aligned(16))) char lds[];
#define STAMP(i)
#define QSTAMP(i)
  const unsigned tid = threadIdx.x, lane = tid & 63u, wid = tid >> 6, l32 = lane & 31u, hi = lane >> 5;
  constexpr int T = NT;
  W4C c;
  c.ldsb = (unsigned)(size_t)lds;
  #pragma unroll
  for (int j = 0; j < 4; ++j) c.vkb[j] = c.ldsb + KRING + 256u * l32 + 16u * ((2u * j + hi) ^ (l32 & 7u));
  c.vvb = c.ldsb + VRING + (((lane & 3u) << 3) | (((lane >> 2) & 3u) << 6) | (((lane >> 4) & 1u) << 5) | (hi << 8));
  #pragma unroll
  for (int par = 0; par < 2; ++par) c.vksrc[par] = KRS * (lane >> 4) + 16u * ((lane & 15u) ^ (4u * par + (lane >> 4)));
  c.vvsrc = KRS * ((lane >> 2) & 7u) + 64u * hi + 16u * (lane & 3u);
  c.m0k = rfl(c.ldsb + KRING + 4096u * wid); c.m0v = rfl(c.ldsb + VRING + 4096u * wid);
  c.sofk = rfl(KRS * 16u * wid); c.sofv = rfl(KRS * 16u * wid);

  const unsigned ublk = blockIdx.x;
  const unsigned xg = ublk & 7u, idx = ublk >> 3;
  const unsigned h = xg * 4u + (idx & 3u), y = idx >> 2;
  const unsigned row0 = 256u * y + 64u * rfl(wid);
  const char* Kbase = (const char*)Kb + (size_t)h * (S * 256);
  const char* Vbase = (const char*)Vb + (size_t)h * (S * 256);
  c.srdK = __builtin_amdgcn_make_buffer_rsrc((void*)Kbase, (short)0, S * 256, SRD_WORD3);
  c.srdV = __builtin_amdgcn_make_buffer_rsrc((void*)Vbase, (short)0, S * 256, SRD_WORD3);
  {
    const unsigned wu = rfl(wid);
    const __amdgpu_buffer_rsrc_t srdX = __builtin_amdgcn_make_buffer_rsrc((void*)Xb, (short)0, S * DM * 2, SRD_WORD3);
    const __amdgpu_buffer_rsrc_t srdW = __builtin_amdgcn_make_buffer_rsrc((void*)Wq, (short)0, DM * NH * D * 2, SRD_WORD3);
    const __amdgpu_buffer_rsrc_t srdM = __builtin_amdgcn_make_buffer_rsrc((void*)modf, (short)0, 2 * DM * 4, SRD_WORD3);
    unsigned vxs[2];
    #pragma unroll
    for (int par = 0; par < 2; ++par) vxs[par] = (lane >> 3) * (DM * 2u) + 16u * ((lane & 7u) ^ ((lane >> 4) + 4u * par));
    const unsigned vws = QRS * ((lane >> 2) & 7u) + 64u * hi + 16u * (lane & 3u);
    auto piece = [&](const int j, const int p) {
      const unsigned slot = c.ldsb + QP_SLOT * (unsigned)((j + 1) % 3);
      if (j < 8) {
        if (p < 8) qdma(slot + (64u * wu + 8u * p) * 128u, vxs[p & 1], srdX, (row0 + 8u * p) * (DM * 2u) + 128u * j);
        else { const int q = p - 8; qdma(slot + 32768u + (4u * wu + q) * 1024u, vws, srdW, (64u * j + 8u * (2u * wu + (q >> 1))) * QRS + h * 256u + 128u * (q & 1)); }
      } else {
        const int q = p & 3;
        if (p < 4) qdma(c.m0k + 1024u * q, c.vksrc[q & 1], c.srdK, c.sofk + KRS * 4u * q);
        else if (p < 8) qdma(c.m0v + 1024u * q, c.vvsrc, c.srdV, c.sofv + KRS * 8u * (q >> 1) + 128u * (q & 1));
        else qdma(c.m0k + 0x4000u + 1024u * q, c.vksrc[q & 1], c.srdK, KTILEB + c.sofk + KRS * 4u * q);
      }
    };
    auto chunk_dma = [&](const int j) {
      #pragma unroll
      for (int p = 0; p < 12; ++p) piece(j, p);
    };
    qdma(c.ldsb + QP_AUX + 1024u * wu, lane * 16u, srdM, 1024u * wu);
    chunk_dma(0); chunk_dma(1);
    const unsigned xrd = (64u * wid + l32) * 128u, xsw = (l32 >> 1) & 7u;
    const unsigned p4 = lane & 3u;
    const unsigned wrd = 32768u + hi * 2048u + ((lane >> 2) & 3u) * 64u + ((lane >> 4) & 1u) * 32u + (((p4 & 1u) << 1) | (p4 >> 1)) * 8u;
    qs16x4 wlo[3][4], wup[3][4]; u32x4 xr[2][2]; f32x4 as[2][4]; u32x4 hw[2][2];
    auto ld1 = [&](auto KC, auto IC) {
      constexpr int k = decltype(KC)::value, i = decltype(IC)::value, j = k >> 2, ks = k & 3;
      if constexpr (k < 32 && i < 14) {
        const char* sb = lds + QP_SLOT * ((j + 1) % 3);
        if constexpr (i < 2) xr[k & 1][i] = *(const u32x4*)(sb + xrd + i * 4096u + (((2u * ks + hi) ^ xsw) << 4));
        else if constexpr (i < 6) as[k & 1][i - 2] = *(const f32x4*)(lds + QP_AUX + 64 * k + hi * 32u + ((i - 2) & 1) * 16 + ((i - 2) >> 1) * 2048);
        else if constexpr (((i - 6) & 1) == 0) wlo[k % 3][(i - 6) >> 1] = __builtin_amdgcn_ds_read_tr16_b64_v4i16((qlds_p)(sb + wrd + ks * 4096 + ((i - 6) >> 1) * 512));
        else wup[k % 3][(i - 6) >> 1] = __builtin_amdgcn_ds_read_tr16_b64_v4i16((qlds_p)(sb + wrd + ks * 4096 + ((i - 6) >> 1) * 512 + 256));
      }
    };
    auto tr1 = [&](auto KC, auto IC) {
      constexpr int k = decltype(KC)::value, i = decltype(IC)::value, qb = i >> 2, e = i & 3;
      if constexpr (k < 32) {
        unsigned r; float t0, t1;
        asm("v_lshlrev_b32 %1, 16, %3\n\tv_and_b32 %2, 0xffff0000, %3\n\tv_fma_f32 %1, %1, %4, %5\n\tv_fma_f32 %2, %2, %6, %7\n\tv_cvt_pk_bf16_f32 %0, %1, %2"
            : "=v"(r), "=&v"(t0), "=&v"(t1)
            : "v"(xr[k & 1][qb][e]), "v"(as[k & 1][2 + (e >> 1)][2 * (e & 1)]), "v"(as[k & 1][e >> 1][2 * (e & 1)]),
              "v"(as[k & 1][2 + (e >> 1)][2 * (e & 1) + 1]), "v"(as[k & 1][e >> 1][2 * (e & 1) + 1]));
        hw[k & 1][qb][e] = r;
      }
    };
    auto gap = [&](auto KC, auto IC) {
      constexpr int k = decltype(KC)::value, i = decltype(IC)::value, db = i >> 1, qb = i & 1;
      const bf16x8 wf = __builtin_bit_cast(bf16x8, __builtin_shufflevector(wlo[k % 3][db], wup[k % 3][db], 0, 1, 2, 3, 4, 5, 6, 7));
      qp_mfma<db, qb, k == 0>(wf, __builtin_bit_cast(bf16x8, hw[k & 1][qb]));
      ld1(std::integral_constant<int, k + 2>{}, std::integral_constant<int, 2 * i>{});
      ld1(std::integral_constant<int, k + 2>{}, std::integral_constant<int, 2 * i + 1>{});
      tr1(std::integral_constant<int, k + 1>{}, IC);
      if constexpr ((k + 2) % 4 == 0 && k >= 2 && k <= 22) piece((k + 2) / 4 + 2, i);
      if constexpr ((k + 1) % 4 == 0 && k >= 3 && k <= 23 && i < 4) piece((k + 1) / 4 + 2, 8 + i);
      GAP();
    };
    auto kstep = [&](auto KC) {
      constexpr int k = decltype(KC)::value;
      if constexpr ((k + 2) % 4 == 0 && k + 2 < 32) {
        constexpr int j = (k + 2) / 4;
        __builtin_amdgcn_s_waitcnt(0xC07F);
        asm volatile("s_waitcnt vmcnt(12)\n\ts_barrier" ::: "memory");
        QSTAMP(j);
        GAP();
      }
      [&]<int... Is>(iseq<Is...>) { (gap(KC, std::integral_constant<int, Is>{}), ...); }(make_iseq<8>{});
    };
    asm volatile("s_waitcnt vmcnt(12)\n\ts_barrier" ::: "memory");
    QSTAMP(0);
    chunk_dma(2);
    [&]<int... Is>(iseq<Is...>) { (ld1(std::integral_constant<int, 0>{}, std::integral_constant<int, Is>{}), ...); }(make_iseq<14>{});
    [&]<int... Is>(iseq<Is...>) { (ld1(std::integral_constant<int, 1>{}, std::integral_constant<int, Is>{}), ...); }(make_iseq<14>{});
    [&]<int... Is>(iseq<Is...>) { (tr1(std::integral_constant<int, 0>{}, std::integral_constant<int, Is>{}), ...); }(make_iseq<8>{});
    GAP();
    [&]<int... Ks>(iseq<Ks...>) { (kstep(std::integral_constant<int, Ks>{}), ...); }(make_iseq<32>{});
    QSTAMP(8);
    asm volatile("s_nop 7\n\ts_nop 7" ::: "memory");
    #pragma unroll
    for (int qb = 0; qb < 2; ++qb)
      #pragma unroll
      for (int db = 0; db < 4; ++db) {
        float o[16];
        W4_READ_O(db, qb, o);
        #pragma unroll
        for (int sq = 0; sq < 2; ++sq) {
          u32x4 w;
          #pragma unroll
          for (int e = 0; e < 4; ++e) w[e] = cvtpk(o[8 * sq + 2 * e], o[8 * sq + 2 * e + 1]);
          q_write(qb, 2 * db + sq, w);
        }
      }
  }
  asm volatile("s_waitcnt vmcnt(0)\n\ts_barrier" ::: "memory");
  {
    const unsigned gw = rfl(blockIdx.x * 4u + wid);
    #pragma unroll
    for (int rep = 0; rep < 2; ++rep) {
      const unsigned u = gw + 1024u * rep;
      const unsigned short* base = u < 512u ? Wo : (u < 684u ? Wg : (u < 856u ? Wu : Wd));
      const unsigned off = u < 512u ? u : (u < 684u ? u - 512u : (u < 856u ? u - 684u : u - 856u));
      const unsigned nbytes = u < 512u ? NH * D * DM * 2u : DM * DF * 2u;
      if (u < 1028u) {
        const __amdgpu_buffer_rsrc_t srdP = __builtin_amdgcn_make_buffer_rsrc((void*)base, (short)0, (int)nbytes, SRD_WORD3);
        asm volatile("s_mov_b32 m0, %0\n\ts_nop 3\n\tbuffer_load_dword %1, %2, %3 offen lds" :: "s"(c.ldsb + QP_AUX + 256u * rfl(wid)), "v"(lane * 128u), "s"(srdP), "s"(off * 8192u) : "m0", "memory");
      }
    }
  }
  if (idx < 1u) {
    unsigned long long pc; unsigned delta;
    asm volatile("s_getpc_b64 %0\n.Lpc_anchor:\n\ts_mov_b32 %1, _Z6post_k8PostArgs - .Lpc_anchor" : "=s"(pc), "=s"(delta));
    pc += (unsigned long long)(long long)(int)delta;
    const __amdgpu_buffer_rsrc_t srdC = __builtin_amdgcn_make_buffer_rsrc((void*)pc, (short)0, POST_CODE_BYTES, SRD_WORD3);
    asm volatile("s_mov_b32 m0, %0\n\ts_nop 3\n\tbuffer_load_dword %1, %2, %3 offen lds" :: "s"(c.ldsb + QP_AUX + 1024u + 256u * rfl(wid)), "v"(lane * 128u), "s"(srdC), "s"((idx * 4u + rfl(wid)) * 8192u) : "m0", "memory");
  }
  k_reads(c, 0u, make_iseq<16>{});
  asm volatile("s_waitcnt lgkmcnt(0)\n\ts_nop 1" ::: "memory");

  STAMP(1);
  typedef float f32x8 __attribute__((ext_vector_type(8)));
  typedef unsigned u32x8 __attribute__((ext_vector_type(8)));
  f32x8 st;
  {
    const u32x8 ad = {c.vkb[0], c.vkb[1], c.vkb[2], c.vkb[3], c.vvb, c.vksrc[0], c.vksrc[1], c.vvsrc};
    unsigned sok = c.sofk + 2u * KTILEB, sov = c.sofv + KTILEB, sov2 = c.sofv + KTILEB + 128u, cnt = T / 2, pend = 0;
    asm volatile(ATTN_CORE_ASM
      : "={v[232:239]}"(st), [sok] "+s"(sok), [sov] "+s"(sov), [sov2] "+s"(sov2), [cnt] "+s"(cnt), [pend] "+s"(pend)
      : "{v[224:231]}"(ad), [srdk] "s"(c.srdK), [srdv] "s"(c.srdV), [m0k0] "s"(c.m0k), [m0k1] "s"(c.m0k + 0x4000u), [m0v0] "s"(c.m0v), [m0v1] "s"(c.m0v + 0x4000u), [thr] "s"(THR)
      : ATTN_CORE_VCLOB, W4_ACLOB, W4_ACLOB_HI, "vcc", "scc", "m0", "memory");
  }
  c.lsum[0] = st[6]; c.lsum[1] = st[7];
  STAMP(2);
  STAMP(3);
  {
    unsigned lb;
    asm volatile("v_mbcnt_lo_u32_b32 %0, -1, 0\n\tv_mbcnt_hi_u32_b32 %0, -1, %0" : "=v"(lb));
    const unsigned l32b = lb & 31u, hib = lb >> 5;
    char* stg = lds + QSTAGE + 16384u * wid;
    #pragma unroll
    for (int qb = 0; qb < 2; ++qb) {
      const float il = __builtin_amdgcn_rcpf(c.lsum[qb]);
      const unsigned r = 32u * qb + l32b;
      #pragma unroll
      for (int db = 0; db < 4; ++db) {
        float o[16];
        W4_READ_O(db, qb, o);
        #pragma unroll
        for (int gq = 0; gq < 4; ++gq) {
          u32x2 w = {cvtpk(o[4 * gq + 0] * il, o[4 * gq + 1] * il), cvtpk(o[4 * gq + 2] * il, o[4 * gq + 3] * il)};
          const unsigned ch = 4u * db + gq;
          *(u32x2*)(stg + 256u * r + 16u * (ch ^ (r & 15u)) + 8u * hib) = w;
        }
      }
    }
    asm volatile("s_waitcnt lgkmcnt(0)" ::: "memory");
    const unsigned cch = lb & 15u, rsb = lb >> 4;
    char* obase = (char*)O + (size_t)row0 * QRS + h * 256u;
    #pragma unroll
    for (int j = 0; j < 16; ++j) {
      const unsigned r = 4u * j + rsb;
      const u32x4 v = *(const u32x4*)(stg + 256u * r + 16u * (cch ^ (r & 15u)));
      *(u32x4*)(obase + (size_t)(4 * j) * QRS + (rsb * QRS + 16u * cch)) = v;
    }
  }
}

typedef short s16x8 __attribute__((ext_vector_type(8)));
typedef short s16x4 __attribute__((ext_vector_type(4)));
DI float bf2f(unsigned short u) { return __uint_as_float((unsigned)u << 16); }
DI float bflo(unsigned w) { return __uint_as_float(w << 16); }
DI float bfhi(unsigned w) { return __uint_as_float(w & 0xffff0000u); }
DI float rbf(float x) { return bflo(cvtpk(x, 0.f)); }

DI float wave_sum(float v) {
  #pragma unroll
  for (int o = 32; o > 0; o >>= 1) v += __shfl_xor(v, o);
  return v;
}
__global__ __launch_bounds__(256) void k_mod(const unsigned short* __restrict__ cvec, const unsigned short* __restrict__ wmod, float* __restrict__ modf,
                                             const unsigned short* __restrict__ x, unsigned short* __restrict__ xn, const unsigned short* __restrict__ wq, unsigned* __restrict__ cnt) {
  __shared__ float red[64][33];
  if (blockIdx.x == 0) for (int i = threadIdx.x; i < 48 * 32; i += 256) cnt[i] = 0u;
  unsigned pfv = 0;
  { const unsigned gw = blockIdx.x * 4u + (threadIdx.x >> 6);
    if (gw < 512u) pfv = __builtin_nontemporal_load((const unsigned*)((const char*)wq + (size_t)gw * 8192u + (threadIdx.x & 63u) * 128u)); }
  if (blockIdx.x >= 96) {
    const int lane = threadIdx.x & 63, row0 = (blockIdx.x - 96) * 16 + (threadIdx.x >> 6) * 4;
    u32x4 xv[4];
    #pragma unroll
    for (int r = 0; r < 4; ++r) xv[r] = *(const u32x4*)(x + (size_t)(row0 + r) * DM + lane * 8);
    #pragma unroll
    for (int r = 0; r < 4; ++r) {
      float v[8];
      #pragma unroll
      for (int j = 0; j < 4; ++j) { v[2 * j] = bflo(xv[r][j]); v[2 * j + 1] = bfhi(xv[r][j]); }
      float s = 0.f;
      #pragma unroll
      for (int j = 0; j < 8; ++j) s += v[j];
      const float mean = wave_sum(s) * (1.f / DM);
      float q = 0.f;
      #pragma unroll
      for (int j = 0; j < 8; ++j) { v[j] -= mean; q += v[j] * v[j]; }
      const float rstd = rsqrtf(wave_sum(q) * (1.f / DM) + 1e-5f);
      u32x4 o;
      #pragma unroll
      for (int j = 0; j < 4; ++j) o[j] = cvtpk(v[2 * j] * rstd, v[2 * j + 1] * rstd);
      *(u32x4*)(xn + (size_t)(row0 + r) * DM + lane * 8) = o;
    }
    asm volatile("" :: "v"(pfv));
    return;
  }
  const int t = threadIdx.x, cg = t & 3, kr = t >> 2, n0 = blockIdx.x * 32;
  float acc[8];
  #pragma unroll
  for (int j = 0; j < 8; ++j) acc[j] = 0.f;
  u32x4 w[8]; float s[8];
  #pragma unroll
  for (int i = 0; i < 8; ++i) w[i] = *(const u32x4*)(wmod + (size_t)(kr + 64 * i) * (6 * DM) + n0 + cg * 8);
  #pragma unroll
  for (int i = 0; i < 8; ++i) { const float x = bf2f(cvec[kr + 64 * i]); s[i] = rbf(x / (1.f + __expf(-x))); }
  #pragma unroll
  for (int i = 0; i < 8; ++i) {
    #pragma unroll
    for (int j = 0; j < 4; ++j) { acc[2 * j] += s[i] * bflo(w[i][j]); acc[2 * j + 1] += s[i] * bfhi(w[i][j]); }
  }
  #pragma unroll
  for (int j = 0; j < 8; ++j) red[kr][cg * 8 + j] = acc[j];
  __syncthreads();
  {
    float p = 0.f;
    #pragma unroll
    for (int k = 0; k < 8; ++k) p += red[(t >> 5) * 8 + k][t & 31];
    __syncthreads();
    red[t >> 5][t & 31] = p;
    __syncthreads();
  }
  if (t < 32) {
    float a = 0.f;
    #pragma unroll
    for (int k = 0; k < 8; ++k) a += red[k][t];
    a = rbf(a);
    const int n = n0 + t, slot = n >> 9;
    if (slot == 1 || slot == 4) a = rbf(1.f + a);
    modf[n] = a;
    if (slot < 2) modf[6 * DM + n] = a * 0.12751743082f;
  }
  asm volatile("" :: "v"(pfv));
}

constexpr int OSPLIT = 4;
DI void dma16(unsigned m0v, unsigned voff, __amdgpu_buffer_rsrc_t srd, unsigned soff) {
  asm volatile("s_mov_b32 m0, %0\n\ts_nop 3\n\tbuffer_load_dwordx4 %1, %2, %3 offen lds" :: "s"(m0v), "v"(voff), "s"(srd), "s"(soff) : "m0", "memory");
}
DI void dma16_sc1(unsigned m0v, unsigned voff, __amdgpu_buffer_rsrc_t srd, unsigned soff) {
  asm volatile("s_mov_b32 m0, %0\n\ts_nop 3\n\tbuffer_load_dwordx4 %1, %2, %3 offen sc1 lds" :: "s"(m0v), "v"(voff), "s"(srd), "s"(soff) : "m0", "memory");
}
template <int N> DI void wait_vm() { asm volatile("s_waitcnt vmcnt(%0)" :: "i"(N) : "memory"); }
typedef __attribute__((address_space(3))) s16x4* lds_s16x4_p;
DI s16x4 trread(const char* p) { return __builtin_amdgcn_ds_read_tr16_b64_v4i16((lds_s16x4_p)(p)); }
DI float geglu(float g, float u) {
  const float p = __builtin_fmaf(g * g, -0.10294324f  , -2.30220819f  );
  return g * __builtin_amdgcn_rcpf(1.f + __builtin_amdgcn_exp2f(g * p)) * u;
}

struct GP {
  const unsigned short* X; const unsigned short* W0; const unsigned short* W1;
  unsigned short* out; const unsigned short* res; const float* gate;
  unsigned ldx, ldw, ldo;
  unsigned xbytes, wbytes, obytes;
  unsigned nvalid;
};
typedef __attribute__((address_space(1))) unsigned gu32;
#define RLX_AGENT __ATOMIC_RELAXED, __HIP_MEMORY_SCOPE_AGENT
DI void wg_signal(unsigned* cnt) {
  asm volatile("s_waitcnt vmcnt(0)" ::: "memory");
  __syncthreads();
  if (threadIdx.x == 0) __hip_atomic_fetch_add((gu32*)cnt, 1u, RLX_AGENT);
}
DI void wg_wait(unsigned* cnt, unsigned want) {
  if (threadIdx.x == 0) {
    for (unsigned spins = 0; spins < (1u << 22); ++spins) {
      if (__hip_atomic_load((gu32*)cnt, RLX_AGENT) >= want) break;
      __builtin_amdgcn_s_sleep(1);
    }
  }
  __syncthreads();
}
constexpr int younger_pieces(int first, int last, int nst_ring, int xpw, int ppw) { int n = 0; for (int s = first; s <= last; ++s) n += s < nst_ring ? xpw : ppw; return n; }
template <int BM, int NMAT, int NBM, int NWM, int NWN, int KS, int NST, int EPI, int NSTG, int KLAST, bool SC1, class WaitF>
DI void gemm_body(char* lds, const GP& a, const unsigned tm, const unsigned tn, const unsigned sp, WaitF&& waitf) {
  constexpr int NW = NWM * NWN * KS, NB = NMAT * NBM;
  constexpr int XB = BM * 128, WB1 = NBM * 4096, STAGE = XB + NMAT * WB1;
  constexpr int XPW = BM / 8 / NW, WPW = 4 * NB / NW, PPW = XPW + WPW;
  constexpr int MBW = BM / 32 / NWM, NBW = NB / NWN;
  static_assert(NW == 8 && BM / 8 % NW == 0 && 4 * NB % NW == 0 && NB % NWN == 0, "piece split");
  static_assert(NMAT == 1 ? (NBM % 2 == 0) : (NBM == 3 && NWN == 2 && KS == 1), "W piece mapping");
  static_assert(NST <= NSTG && KLAST % KS == 0, "ring depth");
  unsigned tid = threadIdx.x; asm volatile("" : "+v"(tid));
  const unsigned lane = tid & 63u, l32 = lane & 31u, hh = lane >> 5;
  const unsigned wid = rfl(tid >> 6), wm = wid % NWM, wn = (wid / NWM) % NWN, kg = wid / (NWM * NWN);
  const unsigned m0 = tm * BM, n0 = tn * (NBM * 32), k0 = sp * NSTG * 64;
  const unsigned ldxb = a.ldx * 2, ldwb = a.ldw * 2;
  const __amdgpu_buffer_rsrc_t srdX = __builtin_amdgcn_make_buffer_rsrc((void*)a.X, (short)0, (int)a.xbytes, SRD_WORD3);
  const unsigned short* Wp = (NMAT == 2 && wid >= 4) ? a.W1 : a.W0;
  const __amdgpu_buffer_rsrc_t srdW = __builtin_amdgcn_make_buffer_rsrc((void*)Wp, (short)0, (int)a.wbytes, SRD_WORD3);
  const unsigned ldsb = (unsigned)(size_t)lds;
  unsigned vx[2];
  #pragma unroll
  for (int j = 0; j < 2; ++j) vx[j] = (lane >> 3) * ldxb + 16u * ((lane & 7u) ^ ((lane >> 4) + 4u * ((wid * XPW + j) & 1u)));
  unsigned vw[3];
  if constexpr (NMAT == 1) { vw[0] = ((lane >> 2) & 7u) * ldwb + 64u * hh + 16u * (lane & 3u); vw[1] = vw[2] = 0; }
  else {
    #pragma unroll
    for (int r = 0; r < 3; ++r) { const unsigned s = 2u * r + hh, kgl = s / 3u, nb = s % 3u; vw[r] = (kgl * 8u + ((lane >> 2) & 7u)) * ldwb + 64u * nb + 16u * (lane & 3u); }
  }
  auto issueX = [&](unsigned st) {
    const unsigned slot = ldsb + (st % NST) * STAGE, kk = k0 + st * 64;
    #pragma unroll
    for (int j = 0; j < XPW; ++j) { const unsigned p = wid * XPW + j;
      if constexpr (EPI >= 2) dma16_sc1(slot + p * 1024u, vx[j & 1], srdX, (m0 + 8u * p) * ldxb + kk * 2u);
      else dma16(slot + p * 1024u, vx[j & 1], srdX, (m0 + 8u * p) * ldxb + kk * 2u); }
  };
  auto issueW = [&](unsigned st) {
    const unsigned slot = ldsb + (st % NST) * STAGE, kk = k0 + st * 64;
    if constexpr (NMAT == 1) {
      #pragma unroll
      for (int j = 0; j < WPW; ++j) { const unsigned pp = wid * WPW + j, kgq = (2u * pp) / NBM, nb = (2u * pp) % NBM;
        dma16(slot + XB + pp * 1024u, vw[0], srdW, (kk + 8u * kgq) * ldwb + (n0 + 32u * nb) * 2u); }
    } else {
      #pragma unroll
      for (int j = 0; j < WPW; ++j) { const unsigned pp = (wid & 3u) * WPW + j;
        dma16(slot + XB + (wid >> 2) * WB1 + pp * 1024u, vw[j % 3], srdW, (kk + 16u * (pp / 3u)) * ldwb + n0 * 2u); }
    }
  };
  constexpr unsigned nst = NSTG;
  #pragma unroll
  for (int s = 0; s < NST; ++s) issueW(s);
  waitf();
#define GST(i)
  GST(0);
  #pragma unroll
  for (int s = 0; s < NST; ++s) issueX(s);
  f32x16 acc[NBW][MBW];
  #pragma unroll
  for (int i = 0; i < NBW; ++i)
    #pragma unroll
    for (int j = 0; j < MBW; ++j) acc[i][j] = f32x16{};
  const unsigned xrd = (wm * (BM / NWM) + l32) * 128u, xsw = (l32 >> 1) & 7u;
  const unsigned wrd = XB + hh * (NBM * 512u) + ((lane >> 2) & 3u) * 64u + ((lane >> 4) & 1u) * 32u + (lane & 3u) * 8u
                     + (NMAT == 2 ? wn * WB1 : wn * (NBW * 512u));
  const bool late = wid >= 4;
  constexpr int KQ = 4 / KS;
  struct Frag { s16x8 x[KQ][MBW]; s16x8 w[KQ][NBW]; };
  auto ldfrag = [&](Frag& f, const int st) {
    const char* sb = lds + (st % NST) * STAGE;
    #pragma unroll
    for (int kq = 0; kq < KQ; ++kq) {
      if (st == NSTG - 1 && kq * KS >= KLAST) continue;
      const unsigned ks = kq * KS + kg;
      #pragma unroll
      for (int mb = 0; mb < MBW; ++mb) f.x[kq][mb] = *(const s16x8*)(sb + xrd + mb * 4096u + (((2u * ks + hh) ^ xsw) << 4));
      #pragma unroll
      for (int i = 0; i < NBW; ++i) {
        const char* wp = sb + wrd + ks * (2 * NBM * 512) + i * 512;
        const s16x4 lo = trread(wp), up = trread(wp + 256);
        f.w[kq][i] = __builtin_shufflevector(lo, up, 0, 1, 2, 3, 4, 5, 6, 7);
      }
    }
  };
  auto mma = [&](const Frag& f, const int st) {
    #pragma unroll
    for (int kq = 0; kq < KQ; ++kq) {
      if (st == NSTG - 1 && kq * KS >= KLAST) continue;
      #pragma unroll
      for (int i = 0; i < NBW; ++i)
        #pragma unroll
        for (int mb = 0; mb < MBW; ++mb) acc[i][mb] = __builtin_amdgcn_mfma_f32_32x32x16_bf16(f.w[kq][i], f.x[kq][mb], acc[i][mb], 0, 0, 0);
    }
  };
  auto step = [&](auto LATE, auto TC, Frag& cur, Frag& nxt) {
    constexpr int t = decltype(TC)::value;
    __builtin_amdgcn_s_waitcnt(0xC07F);
    wait_vm<younger_pieces(t + 2, (t + NST - 1 < NSTG - 1 ? t + NST - 1 : NSTG - 1), NST, XPW, PPW)>();
    __builtin_amdgcn_s_barrier();
    asm volatile("" ::: "memory");
    if constexpr (!LATE.value && t + NST < NSTG) { issueX(t + NST); issueW(t + NST); }
    if constexpr (t + 1 < NSTG) ldfrag(nxt, t + 1);
    mma(cur, t);
    #pragma unroll
    for (int i = 0; i < KQ * NBW * MBW; ++i) {
      __builtin_amdgcn_sched_group_barrier(0x100, (KQ * (MBW + 2 * NBW) + KQ * NBW * MBW - 1) / (KQ * NBW * MBW), 0);
      __builtin_amdgcn_sched_group_barrier(0x8, 1, 0);
    }
    if constexpr (LATE.value && t + NST < NSTG) { issueX(t + NST); issueW(t + NST); }
  };
  Frag fa, fb;
  wait_vm<younger_pieces(1, NST - 1, NST, XPW, PPW)>();
  __builtin_amdgcn_s_barrier();
  asm volatile("" ::: "memory");
  GST(1);
  ldfrag(fa, 0);
  auto run = [&](auto LATE) {
    [&]<int... Ts>(iseq<Ts...>) {
      ((Ts & 1 ? step(LATE, std::integral_constant<int, Ts>{}, fb, fa) : step(LATE, std::integral_constant<int, Ts>{}, fa, fb)), ...);
    }(make_iseq<NSTG>{});
  };
  if (late) run(std::true_type{}); else run(std::false_type{});
  constexpr bool XCH = (KS == 2) || (NMAT == 2);
  GST(2);
  __builtin_amdgcn_s_barrier(); asm volatile("" ::: "memory");
  const bool up_w = wid >= 4;
  if constexpr (XCH) {
    char* xb = lds + wid * (NBW * MBW * 2048) + lane * 16u;
    const char* ib = lds + (wid ^ 4u) * (NBW * MBW * 2048) + lane * 16u;
    auto half1 = [&](auto UP) {
      constexpr int so = UP.value ? 0 : 8;
      #pragma unroll
      for (int i = 0; i < NBW; ++i)
        #pragma unroll
        for (int mb = 0; mb < MBW; ++mb)
          #pragma unroll
          for (int g2 = 0; g2 < 2; ++g2)
            *(f32x4*)(xb + ((i * MBW + mb) * 2 + g2) * 1024) = f32x4{acc[i][mb][so + 4 * g2], acc[i][mb][so + 4 * g2 + 1], acc[i][mb][so + 4 * g2 + 2], acc[i][mb][so + 4 * g2 + 3]};
    };
    auto half2 = [&](auto UP) {
      constexpr int ko = UP.value ? 8 : 0;
      #pragma unroll
      for (int i = 0; i < NBW; ++i)
        #pragma unroll
        for (int mb = 0; mb < MBW; ++mb)
          #pragma unroll
          for (int g2 = 0; g2 < 2; ++g2) {
            const f32x4 u = *(const f32x4*)(ib + ((i * MBW + mb) * 2 + g2) * 1024);
            #pragma unroll
            for (int e = 0; e < 4; ++e) {
              const float own = acc[i][mb][ko + 4 * g2 + e];
              float r;
              if constexpr (EPI == 2) r = UP.value ? geglu(u[e], own) : geglu(own, u[e]);
              else r = own + u[e];
              acc[i][mb][4 * g2 + e] = r;
            }
          }
    };
    if (up_w) half1(std::true_type{}); else half1(std::false_type{});
    __syncthreads();
    if (up_w) half2(std::true_type{}); else half2(std::false_type{});
  }
  GST(3);
  {
    constexpr int BNO = NBM * 32, RS = BNO * 2 + 16, CPR = BNO / 8;
    char* ost = lds + 65536;
    {
      #pragma unroll
      for (int i = 0; i < NBW; ++i)
        #pragma unroll
        for (int mb = 0; mb < MBW; ++mb) {
          char* dst = ost + (wm * (BM / NWM) + 32 * mb + l32) * RS + (32u * ((NMAT == 2 ? 0u : wn * NBW) + i) + 4u * hh) * 2u + ((XCH && up_w) ? 32u : 0u);
          #pragma unroll
          for (int g = 0; g < (XCH ? 2 : 4); ++g)
            *(u32x2*)(dst + 16 * g) = u32x2{cvtpk(acc[i][mb][4 * g], acc[i][mb][4 * g + 1]), cvtpk(acc[i][mb][4 * g + 2], acc[i][mb][4 * g + 3])};
        }
    }
    __syncthreads();
    GST(4);
    const __amdgpu_buffer_rsrc_t srdO = __builtin_amdgcn_make_buffer_rsrc((void*)a.out, (short)0, (int)a.obytes, SRD_WORD3);
    #pragma unroll
    for (unsigned id0 = 0; id0 < BM * CPR; id0 += NW * 64) {
      const unsigned id = id0 + tid, r = id / CPR, ch = id % CPR, n = n0 + 8u * ch;
      if (n < a.nvalid) {
        u32x4 v = *(const u32x4*)(ost + r * RS + ch * 16u);
        const unsigned o = (m0 + r + (EPI == 1 ? sp * SEQ : 0u)) * a.ldo + n;
        if constexpr (EPI == 3) {
          const u32x4 xr = __builtin_amdgcn_raw_buffer_load_b128(__builtin_amdgcn_make_buffer_rsrc((void*)a.res, (short)0, (int)a.obytes, SRD_WORD3), o * 2u, 0, 16);
          const f32x4 g0 = *(const f32x4*)(a.gate + n), g1 = *(const f32x4*)(a.gate + n + 4);
          v[0] = cvtpk(bflo(xr[0]) + g0[0] * bflo(v[0]), bfhi(xr[0]) + g0[1] * bfhi(v[0]));
          v[1] = cvtpk(bflo(xr[1]) + g0[2] * bflo(v[1]), bfhi(xr[1]) + g0[3] * bfhi(v[1]));
          v[2] = cvtpk(bflo(xr[2]) + g1[0] * bflo(v[2]), bfhi(xr[2]) + g1[1] * bfhi(v[2]));
          v[3] = cvtpk(bflo(xr[3]) + g1[2] * bflo(v[3]), bfhi(xr[3]) + g1[3] * bfhi(v[3]));
        }
        __builtin_amdgcn_raw_buffer_store_b128(v, srdO, o * 2u, 0, SC1 ? 16 : 0);
      }
    }
  }
}

DI void ln2_row(const unsigned row, const unsigned lane, const unsigned short* __restrict__ x, const unsigned short* P, const float* __restrict__ modf,
                unsigned short* x1out, unsigned short* h2out) {
  const u32x4 xv = *(const u32x4*)(x + (size_t)row * DM + lane * 8);
  u32x4 pv[OSPLIT];
  const __amdgpu_buffer_rsrc_t srdP = __builtin_amdgcn_make_buffer_rsrc((void*)P, (short)0, OSPLIT * SEQ * DM * 2, SRD_WORD3);
  #pragma unroll
  for (int s = 0; s < OSPLIT; ++s) pv[s] = __builtin_amdgcn_raw_buffer_load_b128(srdP, ((s * SEQ + row) * DM + lane * 8) * 2u, 0, 16);
  const f32x4 g0 = *(const f32x4*)(modf + 2 * DM + lane * 8), g1 = *(const f32x4*)(modf + 2 * DM + lane * 8 + 4);
  const f32x4 a0 = *(const f32x4*)(modf + 4 * DM + lane * 8), a1 = *(const f32x4*)(modf + 4 * DM + lane * 8 + 4);
  const f32x4 s0 = *(const f32x4*)(modf + 3 * DM + lane * 8), s1 = *(const f32x4*)(modf + 3 * DM + lane * 8 + 4);
  f32x4 y0 = {0.f, 0.f, 0.f, 0.f}, y1 = {0.f, 0.f, 0.f, 0.f};
  #pragma unroll
  for (int s = 0; s < OSPLIT; ++s) {
    y0 += f32x4{bflo(pv[s][0]), bfhi(pv[s][0]), bflo(pv[s][1]), bfhi(pv[s][1])};
    y1 += f32x4{bflo(pv[s][2]), bfhi(pv[s][2]), bflo(pv[s][3]), bfhi(pv[s][3])};
  }
  float v[8];
  #pragma unroll
  for (int j = 0; j < 4; ++j) { v[j] = g0[j] * y0[j]; v[4 + j] = g1[j] * y1[j]; }
  u32x4 x1v;
  #pragma unroll
  for (int j = 0; j < 4; ++j) { x1v[j] = cvtpk(bflo(xv[j]) + v[2 * j], bfhi(xv[j]) + v[2 * j + 1]); v[2 * j] = bflo(x1v[j]); v[2 * j + 1] = bfhi(x1v[j]); }
  const __amdgpu_buffer_rsrc_t srd1 = __builtin_amdgcn_make_buffer_rsrc((void*)x1out, (short)0, SEQ * DM * 2, SRD_WORD3);
  const __amdgpu_buffer_rsrc_t srd2 = __builtin_amdgcn_make_buffer_rsrc((void*)h2out, (short)0, SEQ * DM * 2, SRD_WORD3);
  __builtin_amdgcn_raw_buffer_store_b128(x1v, srd1, (row * DM + lane * 8) * 2u, 0, 16);
  float s = 0.f;
  #pragma unroll
  for (int j = 0; j < 8; ++j) s += v[j];
  const float mean = wave_sum(s) * (1.f / DM);
  float q = 0.f;
  #pragma unroll
  for (int j = 0; j < 8; ++j) { v[j] -= mean; q += v[j] * v[j]; }
  const float rstd = rsqrtf(wave_sum(q) * (1.f / DM) + 1e-5f);
  u32x4 o;
  o[0] = cvtpk(v[0] * rstd * a0[0] + s0[0], v[1] * rstd * a0[1] + s0[1]);
  o[1] = cvtpk(v[2] * rstd * a0[2] + s0[2], v[3] * rstd * a0[3] + s0[3]);
  o[2] = cvtpk(v[4] * rstd * a1[0] + s1[0], v[5] * rstd * a1[1] + s1[1]);
  o[3] = cvtpk(v[6] * rstd * a1[2] + s1[2], v[7] * rstd * a1[3] + s1[3]);
  __builtin_amdgcn_raw_buffer_store_b128(o, srd2, (row * DM + lane * 8) * 2u, 0, 16);
}

struct PostArgs {
  const unsigned short *x, *ob, *wo, *wg, *wu, *wd; const float* modf;
  unsigned short *pb, *x1, *h2, *ab, *out; unsigned* cnt;
};
#define GDBG
constexpr int CNT_STRIDE = 32;
constexpr int POST_LDS = 131072;
__global__ __launch_bounds__(512, 1) void post_k(const PostArgs a) {
  extern __shared__ __attribute__((aligned(16))) char lds[];
  const unsigned bid = blockIdx.x, xg = bid & 7u, idx = bid >> 3;
  unsigned* c1 = a.cnt; unsigned* c2 = a.cnt + 16 * CNT_STRIDE; unsigned* c3 = a.cnt + 32 * CNT_STRIDE;
#define PSTAMP(i)
  PSTAMP(0);
  const unsigned tm1 = (xg & 1u) * 8u + (idx & 7u), tn1 = idx >> 3, sp1 = xg >> 1;
  {
    const GP g{a.ob, a.wo, a.wo, a.pb, nullptr, nullptr, NH * DH, DM, DM, SEQ * NH * DH * 2, NH * DH * DM * 2, OSPLIT * SEQ * DM * 2, DM GDBG};
    gemm_body<128, 1, 4, 4, 2, 1, 4, 1, NH * DH / 64 / OSPLIT, 4, true>(lds, g, tm1, tn1, sp1, [] {});
  }
  PSTAMP(1);
  wg_signal(c1 + tm1 * CNT_STRIDE);
  PSTAMP(2);
  auto p2 = [&] {
    wg_wait(c1 + tm1 * CNT_STRIDE, 16u);
    PSTAMP(3);
    ln2_row(tm1 * 128u + (sp1 * 4u + tn1) * 8u + (threadIdx.x >> 6), threadIdx.x & 63u, a.x, a.pb, a.modf, a.x1, a.h2);
    wg_signal(c2 + tm1 * CNT_STRIDE);
    PSTAMP(4);
  };
  const unsigned tm3 = (xg >> 1) * 4u + (idx & 3u), tn3 = (xg & 1u) * 8u + (idx >> 2);
  if (tn3 < 15u) {
    const GP g{a.h2, a.wg, a.wu, a.ab, nullptr, nullptr, DM, DF, DF, SEQ * DM * 2, DM * DF * 2, SEQ * DF * 2, DF GDBG};
    gemm_body<128, 2, 3, 4, 2, 1, 3, 2, DM / 64, 4, true>(lds, g, tm3, tn3, 0u, [&] { p2(); wg_wait(c2 + tm3 * CNT_STRIDE, 16u); PSTAMP(5); });
    PSTAMP(6);
    wg_signal(c3 + tm3 * CNT_STRIDE);
    PSTAMP(7);
  } else {
    p2(); PSTAMP(5); PSTAMP(6); PSTAMP(7);
  }
  const unsigned tm4 = xg * 4u + (idx & 3u), tn4 = idx >> 2;
  {
    const GP g{a.ab, a.wd, a.wd, a.out, a.x1, a.modf + 5 * DM, DF, DM, DM, SEQ * DF * 2, DF * DM * 2, SEQ * DM * 2, DM GDBG};
    gemm_body<64, 1, 2, 2, 2, 2, 8, 3, (DF + 63) / 64, (DF % 64) / 16, false>(lds, g, tm4, tn4, 0u, [&] { wg_wait(c3 + (tm4 >> 1) * CNT_STRIDE, 15u); PSTAMP(8); });
  }
}

constexpr size_t MB = 1u << 20;
constexpr size_t WS_MOD = 0, WS_STAT = 65536, WS_H = 1 * MB, WS_Q = 4 * MB, WS_O = 20 * MB, WS_P = 36 * MB, WS_X1 = 52 * MB, WS_H2 = 54 * MB, WS_A = 56 * MB;

#define DBGP
template <typename K> static void set_lds(K k, int bytes) { (void)hipFuncSetAttribute((const void*)k, hipFuncAttributeMaxDynamicSharedMemorySize, bytes); }

extern "C" void kernel_launch(void* const* d_in, const int* in_sizes, int n_in, void* d_out, int out_size, void* d_ws, size_t ws_size, hipStream_t stream) {
  const unsigned short* x   = (const unsigned short*)d_in[0];
  const unsigned short* kc  = (const unsigned short*)d_in[1];
  const unsigned short* vc  = (const unsigned short*)d_in[2];
  const unsigned short* wq  = (const unsigned short*)d_in[3];
  const unsigned short* wo  = (const unsigned short*)d_in[4];
  const unsigned short* wg  = (const unsigned short*)d_in[6];
  const unsigned short* wu  = (const unsigned short*)d_in[7];
  const unsigned short* wd  = (const unsigned short*)d_in[8];
  const unsigned short* cv  = (const unsigned short*)d_in[9];
  const unsigned short* wm  = (const unsigned short*)d_in[10];
  char* ws = (char*)d_ws;
  float* modf = (float*)(ws + WS_MOD);
  unsigned short* hb = (unsigned short*)(ws + WS_H);
  unsigned short* ob = (unsigned short*)(ws + WS_O);
  unsigned short* pb = (unsigned short*)(ws + WS_P);
  unsigned short* x1 = (unsigned short*)(ws + WS_X1);
  unsigned short* h2 = (unsigned short*)(ws + WS_H2);
  unsigned short* ab = (unsigned short*)(ws + WS_A);

  unsigned* cnt = (unsigned*)(ws + WS_STAT);
  k_mod<<<96 + SEQ / 16, 256, 0, stream>>>(cv, wm, modf, x, hb, wq, cnt);
  set_lds(attn_kernel, (int)SHM_ATTN);
  attn_kernel<<<256, 256, SHM_ATTN, stream>>>(hb, modf + 6 * DM, wq, kc, vc, ob, wo, wg, wu, wd);
  {
    set_lds(post_k, POST_LDS);
    PostArgs pa{x, ob, wo, wg, wu, wd, modf, pb, x1, h2, ab, (unsigned short*)d_out, cnt};
    post_k<<<256, 512, POST_LDS, stream>>>(pa);
  }
}
```
